```python
import torch

def _softmax(x, axis=-1):
    return torch.softmax(x, dim=axis)
def _gelu(x):
    return torch.nn.functional.gelu(x, approximate="tanh")
def _silu(x):
    return torch.nn.functional.silu(x)
def _sigmoid(x):
    return torch.sigmoid(x)
def _layernorm(x, axis=-1, eps=1e-5):
    m = x.mean(dim=axis, keepdim=True)
    v = x.var(dim=axis, keepdim=True, unbiased=False)
    return (x - m) / torch.sqrt(v + eps)
def _scatter_add(tgt, idx, src, axis=0):
    return tgt.clone().index_add_(axis, idx, src)
def _segment_sum(src, seg, n):
    out = torch.zeros((n,) + src.shape[1:], dtype=src.dtype)
    return out.index_add_(0, seg, src)
def _pad(x, pw):
    flat = tuple(v for pair in reversed(pw) for v in pair)
    return torch.nn.functional.pad(x, flat)

kernel_name = 'recurrent_scan__grouped_kv_mqa__moe_arch_mod__prompt'

def setup_inputs():

    _w = (lambda a: (lambda t: t.to(torch.bfloat16) if t.is_floating_point() else t)(torch.as_tensor(a)))
    def _A_setup():
        B, S, D, H = 1, 2048, 512, 32
        Dh = D // H
        return {
            "x": _w((lambda *s: torch.randn(*s, dtype=torch.bfloat16) * 0.05)(B, S, D)),
            "wq": _w((lambda *s: torch.randn(*s, dtype=torch.bfloat16) * 0.05)(D, H * Dh)),
            "wk": _w((lambda *s: torch.randn(*s, dtype=torch.bfloat16) * 0.05)(D, Dh)),
            "wv": _w((lambda *s: torch.randn(*s, dtype=torch.bfloat16) * 0.05)(D, Dh)),
            "wo": _w((lambda *s: torch.randn(*s, dtype=torch.bfloat16) * 0.05)(H * Dh, D)),
        }
    _A_in = _A_setup()

    def _B_setup():
        B, S, D, F, C = 1, 2048, 512, 1792, 2
        return {
            "x": _w((lambda *s: torch.randn(*s, dtype=torch.bfloat16) * 0.05)(B, S, D)),
            "w_r": _w((lambda *s: torch.randn(*s, dtype=torch.bfloat16) * 1.6)(D)),
            "w_gate": _w((lambda *s: torch.randn(*s, dtype=torch.bfloat16) * 0.1)(D, F)),
            "w_up": _w((lambda *s: torch.randn(*s, dtype=torch.bfloat16) * 0.1)(D, F)),
            "w_down": _w((lambda *s: torch.randn(*s, dtype=torch.bfloat16) * 0.1)(F, D)),
        }
    _B_in = _B_setup()

    return {
        "A_x": _A_in["x"],
        "A_wq": _A_in["wq"],
        "A_wk": _A_in["wk"],
        "A_wv": _A_in["wv"],
        "A_wo": _A_in["wo"],
        "B_x": _B_in["x"],
        "B_w_r": _B_in["w_r"],
        "B_w_gate": _B_in["w_gate"],
        "B_w_up": _B_in["w_up"],
        "B_w_down": _B_in["w_down"],
    }

def reference(A_x, A_wq, A_wk, A_wv, A_wo, B_x, B_w_r, B_w_gate, B_w_up, B_w_down):
    def _A_ref(x, wq, wk, wv, wo):
        B, S, D = x.shape
        H = 32
        Dh = D // H
        q = (lambda x, *d: x.permute(*d))((x @ wq).reshape(B, S, H, Dh), 0, 2, 1, 3)
        k = x @ wk
        v = x @ wv
        scores = torch.einsum("bhqd,bkd->bhqk", q, k) * (Dh ** -0.5)
        p = _softmax(scores, axis=-1)
        o = torch.einsum("bhqk,bkd->bhqd", p, v)
        return (lambda x, *d: x.permute(*d))(o, 0, 2, 1, 3).reshape(B, S, H * Dh) @ wo

    def _B_ref(x, w_r, w_gate, w_up, w_down):
        B, S, D = x.shape
        C = 2
        r = x @ w_r
        idx = torch.argsort(-r, axis=-1)[:, :C]
        idx_e = (lambda x, n, axis: torch.repeat_interleave(x, n, dim=axis))(idx.reshape(B, C, 1), D, axis=2)
        x_sel = (lambda x, i, axis: torch.take_along_dim(x, i, dim=axis))(x, idx_e, axis=1)
        g = _silu(x_sel @ w_gate)
        u = x_sel @ w_up
        y = (g * u) @ w_down
        r_sel = _sigmoid((lambda x, i, axis: torch.take_along_dim(x, i, dim=axis))(r, idx, axis=1))
        y = y * r_sel.reshape(B, C, 1)
        b_off = (torch.arange(B) * S).reshape(B, 1)
        flat_idx = (idx + b_off).reshape(B * C)
        zero = (x * 0.0).reshape(B * S, D)
        upd = _scatter_add(zero, flat_idx, y.reshape(B * C, D), axis=0)
        return x + upd.reshape(B, S, D)

    x = A_x
    xn = _layernorm(x, axis=-1)
    s = _A_ref(xn, A_wq, A_wk, A_wv, A_wo)
    g = _silu(_B_ref(xn, B_w_r, B_w_gate, B_w_up, B_w_down))
    return x + s * g
```

```cpp
#include <hip/hip_runtime.h>
#include <hip/hip_bf16.h>
#include <stdint.h>

typedef __attribute__((ext_vector_type(16))) __bf16 bf16x16;
typedef __attribute__((ext_vector_type(8))) __bf16 bf16x8;
typedef __attribute__((ext_vector_type(4))) __bf16 bf16x4;
typedef __attribute__((ext_vector_type(2))) __bf16 bf16x2;
typedef __attribute__((ext_vector_type(16))) float f32x16;
typedef __attribute__((ext_vector_type(8))) float f32x8;
typedef __attribute__((ext_vector_type(4))) float f32x4;
typedef __attribute__((ext_vector_type(2))) float f32x2;
typedef __attribute__((ext_vector_type(4))) unsigned u32x4;
typedef __attribute__((ext_vector_type(2))) unsigned u32x2;
#define LAS __attribute__((address_space(3)))
#define DI __device__ __forceinline__
#define BC(T, v) __builtin_bit_cast(T, v)
#define MFMA32(a, b, c) __builtin_amdgcn_mfma_f32_32x32x16_bf16((a), (b), (c), 0, 0, 0)
#define MFMA16(a, b, c) __builtin_amdgcn_mfma_f32_16x16x32_bf16((a), (b), (c), 0, 0, 0)
#define SMF(a, b8, c) __builtin_amdgcn_smfmac_f32_32x32x32_bf16((a), BC(bf16x16, b8), (c), (int)0xDDDDDDDD, 0, 0)
#define SB __builtin_amdgcn_sched_barrier(0)

constexpr int S_ = 2048, D_ = 512, F_ = 1792;
constexpr size_t WS_Q = 0;
constexpr size_t WS_KV = 2u << 20;
constexpr size_t WS_WO = WS_KV + 131072;
constexpr size_t WS_STATS = WS_WO + 524288;
constexpr size_t WS_KEYS = WS_STATS + 16384;
constexpr size_t WS_KN = WS_KEYS + 8192;
constexpr size_t WS_SEL = WS_KN + 1024;
constexpr size_t WS_YP = WS_SEL + 256;

DI float rbf(float x) { return (float)(__bf16)x; }
DI unsigned pk2(float a, float b) { f32x2 v = {a, b}; return BC(unsigned, __builtin_convertvector(v, bf16x2)); }
DI float bflo(unsigned u) { return BC(float, u << 16); }
DI float bfhi(unsigned u) { return BC(float, u & 0xffff0000u); }
DI float dot2(unsigned a, unsigned b, float c) { return __builtin_amdgcn_fdot2_f32_bf16(BC(bf16x2, a), BC(bf16x2, b), c, false); }
DI float halfsum(float v) { auto r = __builtin_amdgcn_permlane32_swap(BC(unsigned, v), BC(unsigned, v), false, false); const unsigned a = r[0], b = r[1]; return BC(float, a) + BC(float, b); }
DI float halfmax(float v) { auto r = __builtin_amdgcn_permlane32_swap(BC(unsigned, v), BC(unsigned, v), false, false); const unsigned a = r[0], b = r[1]; return fmaxf(BC(float, a), BC(float, b)); }
template <int CTRL, int RM = 0xf> DI float dppf(float x) { return BC(float, __builtin_amdgcn_update_dpp(0, BC(int, x), CTRL, RM, 0xf, true)); }
DI float rowsum16(float x) { x += dppf<0xB1>(x); x += dppf<0x4E>(x); x += dppf<0x141>(x); x += dppf<0x140>(x); return x; }
DI float rowmax16(float x) { x = fmaxf(x, dppf<0xB1>(x)); x = fmaxf(x, dppf<0x4E>(x)); x = fmaxf(x, dppf<0x141>(x)); x = fmaxf(x, dppf<0x140>(x)); return x; }
DI float x16sum(float v) { auto r = __builtin_amdgcn_permlane16_swap(BC(unsigned, v), BC(unsigned, v), false, false); const unsigned a = r[0], b = r[1]; return BC(float, a) + BC(float, b); }
DI float x16max(float v) { auto r = __builtin_amdgcn_permlane16_swap(BC(unsigned, v), BC(unsigned, v), false, false); const unsigned a = r[0], b = r[1]; return fmaxf(BC(float, a), BC(float, b)); }
DI float wavesum(float x) { return halfsum(x16sum(rowsum16(x))); }
DI float wavemax(float x) { return halfmax(x16max(rowmax16(x))); }

DI void glds16(const void* gsrc, unsigned lds_dst) {
  unsigned keep;
  asm volatile("s_mov_b32 %0, m0\n\ts_mov_b32 m0, %2\n\ts_nop 0\n\tglobal_load_lds_dwordx4 %1, off\n\ts_mov_b32 m0, %0"
               : "=&s"(keep) : "v"(gsrc), "s"(lds_dst) : "memory");
}
#define VMWAIT(n) asm volatile("s_waitcnt vmcnt(" #n ")" ::: "memory")
#define BARRIER() do { asm volatile("s_waitcnt lgkmcnt(0)" ::: "memory"); __builtin_amdgcn_s_barrier(); asm volatile("" ::: "memory"); } while (0)

DI bf16x8 trfrag(const char* p) {
  return bf16x8{};
}
DI bf16x4 trread(const char* p) { return __builtin_amdgcn_ds_read_tr16_b64_v4bf16((LAS bf16x4*)p); }
DI bf16x8 cat(bf16x4 a, bf16x4 b) { return __builtin_shufflevector(a, b, 0, 1, 2, 3, 4, 5, 6, 7); }

DI void ln_stats(float s1, float s2, float& m, float& sq) {
  float mean = s1 * (1.0f / 512.0f);
  float var = fmaxf(s2 * (1.0f / 512.0f) - mean * mean, 0.0f);
  m = rbf(mean);
  float ve = rbf(rbf(var) + 1e-5f);
  sq = rbf(__builtin_sqrtf(ve));
}

constexpr int K1_XI = 0, K1_WI = 65536, K1_KVI = 131072, K1_LDS = 147456;
constexpr int K1_PART = K1_XI;
constexpr int K1_KVP = K1_XI + 16384;
constexpr int K1_S1 = K1_WI;
constexpr int K1_S2 = K1_WI + 1024;
constexpr int K1_CS = K1_WI + 2048;
constexpr int K1_XR = K1_WI + 2560;

__global__ __launch_bounds__(512, 2) void k1_proj(const char* __restrict__ x, const char* __restrict__ wq, const char* __restrict__ wk,
                                                  const char* __restrict__ wv, const char* __restrict__ wo, const char* __restrict__ wr,
                                                  char* __restrict__ ws) {
  extern __shared__ __attribute__((aligned(16))) char smem[];
  const int tid = threadIdx.x, lane = tid & 63, w = __builtin_amdgcn_readfirstlane(tid >> 6);
  const int b = blockIdx.x, rt = (b & 7) * 4 + (b >> 6), cb = (b >> 3) & 7;
  const int row0 = rt * 64, col0 = cb * 64;
  const unsigned lds0 = (unsigned)(size_t)smem;
  const int kh = w >> 2, mn = (w & 3) ^ kh, mh = mn >> 1, nh = mn & 1;
  const bool heavy = (mh == nh);
  const int lw = kh * 2 + mh;
  const int slab = cb >> 1, which = cb & 1;

  const u32x4 xrow = *(const u32x4*)(x + (size_t)(row0 + cb * 8 + w) * 1024 + lane * 16);
  const u32x4 wrv = *(const u32x4*)(wr + lane * 16);
  u32x4 wrow[8];
  const bool wojob = (w == 5) && (cb < 2);
  const int job = rt * 2 + cb, jks = job >> 2, jcr = job & 3;
  if (wojob) {
    const int g = lane >> 4, c = lane & 15;
#pragma unroll
    for (int j = 0; j < 8; ++j) {
      const int k = (2 * jks + (g >> 1)) * 16 + 4 * (g & 1) + (j & 3) + 8 * (j >> 2);
      wrow[j] = *(const u32x4*)(wo + (size_t)k * 1024 + jcr * 256 + c * 16);
    }
  }
  {
    const char* wkv = which ? wv : wk;
#pragma unroll
    for (int c = 0; c < 4; ++c) {
#pragma unroll
      for (int i = 0; i < 2; ++i) {
        const int p = w * 2 + i, row = 4 * p + (lane >> 4), slot = lane & 15;
        glds16(x + (size_t)(row0 + row) * 1024 + c * 256 + ((slot ^ (row & 15)) << 4), lds0 + K1_XI + c * 16384 + p * 1024);
      }
#pragma unroll
      for (int i = 0; i < 2; ++i) {
        const int p = w * 2 + i, k = c * 128 + 8 * p + (lane >> 3), slot = lane & 7;
        glds16(wq + (size_t)k * 1024 + col0 * 2 + ((slot ^ (((k >> 1) & 1) << 2)) << 4), lds0 + K1_WI + c * 16384 + p * 1024);
      }
      if (c == 0) {
#pragma unroll
        for (int i = 0; i < 2; ++i) { const int p = w * 2 + i; glds16(wkv + p * 1024 + lane * 16, lds0 + K1_KVI + p * 1024); }
      }
    }
  }
  const int r = lane & 31, h = lane >> 5, gi = lane >> 4, i16 = lane & 15, q4 = i16 >> 2, p4 = i16 & 3;
  const int xrow_l = mh * 32 + r;
  const char* xa = smem + K1_XI + xrow_l * 256;
  const int a0 = (h ^ (xrow_l & 15)) << 4;
  const int wslot = (nh * 4 + (gi & 1) * 2 + (p4 >> 1)) ^ (((q4 >> 1) & 1) << 2);
  const char* wa = smem + K1_WI + (8 * (gi >> 1) + q4) * 128 + wslot * 16 + (p4 & 1) * 8 + kh * 2048;
  const int kvrow = slab * 16 + i16;
  const char* xk = smem + K1_XI + kvrow * 256 + (((4 * lw + gi) ^ (kvrow & 15)) << 4);
  const char* kva = smem + K1_KVI + (lw * 32 + 8 * gi + q4) * 32 + 8 * p4;
  f32x16 acc; for (int i = 0; i < 16; ++i) acc[i] = 0.f;
  f32x4 kacc = {0.f, 0.f, 0.f, 0.f}, kcs = {0.f, 0.f, 0.f, 0.f};
  float st = 0.f, cs = 0.f;
  const unsigned ONE2 = 0x3f803f80u;
  const u32x4 ones4 = {ONE2, ONE2, ONE2, ONE2};
#pragma unroll
  for (int c = 0; c < 4; ++c) {
    if (c == 0) VMWAIT(12); else if (c == 1) VMWAIT(8); else if (c == 2) VMWAIT(4); else VMWAIT(0);
    BARRIER();
#pragma unroll
    for (int i = 0; i < 4; ++i) {
      const int ks = 2 * i;
      const u32x4 xf = *(const u32x4*)(xa + c * 16384 + (a0 ^ ((ks + kh) << 5)));
      const bf16x8 wf = cat(trread(wa + c * 16384 + ks * 2048), trread(wa + c * 16384 + ks * 2048 + 512));
      acc = MFMA32(wf, BC(bf16x8, xf), acc);
      const u32x4 wu = BC(u32x4, wf);
      if (heavy) {
#pragma unroll
        for (int e = 0; e < 4; ++e) { st = dot2(xf[e], ONE2, st); cs = dot2(wu[e], ONE2, cs); }
      } else {
#pragma unroll
        for (int e = 0; e < 4; ++e) st = dot2(xf[e], xf[e], st);
      }
    }
    if (!heavy) {
      const bf16x8 xf = *(const bf16x8*)(xk + c * 16384);
      const bf16x8 wf = cat(trread(kva + c * 4096), trread(kva + c * 4096 + 128));
      if (which == 0) { kacc = MFMA16(wf, xf, kacc); kcs = MFMA16(wf, BC(bf16x8, ones4), kcs); }
      else            { kacc = MFMA16(xf, wf, kacc); kcs = MFMA16(BC(bf16x8, ones4), wf, kcs); }
    }
  }
  {
    float xr = 0.f, sw = 0.f;
#pragma unroll
    for (int e = 0; e < 4; ++e) { xr = dot2(xrow[e], wrv[e], xr); sw = dot2(wrv[e], ONE2, sw); }
    xr = wavesum(xr); sw = wavesum(sw);
    if (lane == 0) { *(float*)(smem + K1_XR + w * 4) = xr; *(float*)(smem + K1_XR + 32 + w * 4) = sw; }
  }
  *(float*)(smem + (heavy ? K1_S1 : K1_S2) + xrow_l * 16 + (kh * 2 + h) * 4) = st;
  if (heavy) { cs = halfsum(cs); if (h == 0) *(float*)(smem + K1_CS + kh * 256 + (nh * 32 + r) * 4) = cs; }
  else { *(f32x4*)(smem + K1_KVP + lw * 2048 + lane * 16) = kacc; *(f32x4*)(smem + K1_KVP + lw * 2048 + 1024 + lane * 16) = kcs; }
  if (kh == 1) {
#pragma unroll
    for (int g = 0; g < 4; ++g) *(f32x4*)(smem + K1_PART + mn * 4096 + g * 1024 + lane * 16) = f32x4{acc[4 * g], acc[4 * g + 1], acc[4 * g + 2], acc[4 * g + 3]};
  }
  BARRIER();
  if (kh == 0) {
    const f32x4 s1 = *(const f32x4*)(smem + K1_S1 + xrow_l * 16), s2 = *(const f32x4*)(smem + K1_S2 + xrow_l * 16);
    float m, sq; ln_stats(s1[0] + s1[1] + s1[2] + s1[3], s2[0] + s2[1] + s2[2] + s2[3], m, sq);
    const float sc = (0.25f * 1.4426950408889634f) / sq;
    float v[16];
#pragma unroll
    for (int g = 0; g < 4; ++g) {
      const f32x4 p = *(const f32x4*)(smem + K1_PART + mn * 4096 + g * 1024 + lane * 16);
      const f32x4 c0 = *(const f32x4*)(smem + K1_CS + (nh * 32 + 8 * g + 4 * h) * 4);
      const f32x4 c1 = *(const f32x4*)(smem + K1_CS + 256 + (nh * 32 + 8 * g + 4 * h) * 4);
#pragma unroll
      for (int e = 0; e < 4; ++e) v[4 * g + e] = (acc[4 * g + e] + p[e] - m * (c0[e] + c1[e])) * sc;
    }
    const int token = row0 + xrow_l;
#pragma unroll
    for (int hh = 0; hh < 2; ++hh) {
      const u32x4 o = {pk2(v[8 * hh], v[8 * hh + 1]), pk2(v[8 * hh + 2], v[8 * hh + 3]), pk2(v[8 * hh + 4], v[8 * hh + 5]), pk2(v[8 * hh + 6], v[8 * hh + 7])};
      *(u32x4*)(ws + WS_Q + (size_t)((((token >> 3) * 8 + cb) * 64) + h * 32 + (nh * 2 + hh) * 8 + (token & 7)) * 16) = o;
    }
  } else if (w == 4 || w == 6) {
    const f32x4 s1 = *(const f32x4*)(smem + K1_S1 + lane * 16), s2 = *(const f32x4*)(smem + K1_S2 + lane * 16);
    float m, sq; ln_stats(s1[0] + s1[1] + s1[2] + s1[3], s2[0] + s2[1] + s2[2] + s2[3], m, sq);
    const float inv = 1.0f / sq;
    if (w == 6) {
      if (cb == 0) *(f32x2*)(ws + WS_STATS + (size_t)(row0 + lane) * 8) = f32x2{m, sq};
      if ((lane >> 3) == cb) {
        const float xr = *(const float*)(smem + K1_XR + (lane & 7) * 4), sw = *(const float*)(smem + K1_XR + 32);
        const float rr = (xr - m * sw) * inv;
        unsigned u = BC(unsigned, rr); u = (u & 0x80000000u) ? ~u : (u | 0x80000000u);
        *(unsigned*)(ws + WS_KEYS + (size_t)(row0 + lane) * 4) = (u & 0xfffff800u) | (unsigned)(2047 - (row0 + lane));
      }
    } else {
      f32x4 a = {0.f, 0.f, 0.f, 0.f}, c = {0.f, 0.f, 0.f, 0.f};
#pragma unroll
      for (int i = 0; i < 4; ++i) { a += *(const f32x4*)(smem + K1_KVP + i * 2048 + lane * 16); c += *(const f32x4*)(smem + K1_KVP + i * 2048 + 1024 + lane * 16); }
      const int key0 = row0 + slab * 16;
      char* tile = ws + WS_KV + (size_t)(key0 >> 5) * 2048;
      if (which == 0) {
        const float mm = __shfl(m, slab * 16 + i16), ii = __shfl(inv, slab * 16 + i16);
        const u32x2 o = {pk2((a[0] - mm * c[0]) * ii, (a[1] - mm * c[1]) * ii), pk2((a[2] - mm * c[2]) * ii, (a[3] - mm * c[3]) * ii)};
        *(u32x2*)(tile + ((gi & 1) * 32 + (key0 & 31) + i16) * 16 + (gi >> 1) * 8) = o;
        float n2 = dot2(o[0], o[0], dot2(o[1], o[1], 0.f));
        n2 = rowmax16(halfsum(x16sum(n2)));
        if (lane == 0) *(float*)(ws + WS_KN + (rt * 4 + slab) * 4) = n2;
      } else {
        float v[4];
#pragma unroll
        for (int e = 0; e < 4; ++e) { const float mm = __shfl(m, slab * 16 + 4 * gi + e), ii = __shfl(inv, slab * 16 + 4 * gi + e); v[e] = (a[e] - mm * c[e]) * ii; }
        const u32x2 o = {pk2(v[0], v[1]), pk2(v[2], v[3])};
        *(u32x2*)(tile + 1024 + (slab & 1) * 512 + ((gi >> 1) * 16 + i16) * 16 + (gi & 1) * 8) = o;
        float mx = wavemax(fmaxf(fmaxf(fabsf(v[0]), fabsf(v[1])), fmaxf(fabsf(v[2]), fabsf(v[3]))));
        if (lane == 0) *(float*)(ws + WS_KN + 512 + (rt * 4 + slab) * 4) = mx;
      }
    }
  } else if (wojob) {
    const int g = lane >> 4, c = lane & 15;
#pragma unroll
    for (int e = 0; e < 8; ++e) {
      u32x4 o;
#pragma unroll
      for (int jp = 0; jp < 4; ++jp) {
        const unsigned lo = wrow[2 * jp][e >> 1], hi = wrow[2 * jp + 1][e >> 1];
        o[jp] = (e & 1) ? __builtin_amdgcn_perm(hi, lo, 0x07060302u) : __builtin_amdgcn_perm(hi, lo, 0x05040100u);
      }
      const int col = jcr * 128 + c * 8 + e, ww = col >> 6, mq = (col >> 4) & 3, i = (col >> 2) & 3, mr = col & 3;
      *(u32x4*)(ws + WS_WO + (size_t)(((ww * 16 + jks) * 4 + i) * 64 + g * 16 + mq * 4 + mr) * 16) = o;
    }
  }
}

__global__ __launch_bounds__(256) void kf_ffn(const char* __restrict__ x, const char* __restrict__ wg, const char* __restrict__ wu,
                                              const char* __restrict__ wd, char* __restrict__ ws) {
  __shared__ unsigned sk[512];
  __shared__ float xs[2][512];
  __shared__ float red[16][16][8];
  __shared__ float hs[2][32];
  const int tid = threadIdx.x;
  unsigned m1 = 0, m2 = 0;
  for (int i = 0; i < 8; ++i) { unsigned k = *(const unsigned*)(ws + WS_KEYS + (size_t)(tid * 8 + i) * 4); m2 = max(m2, min(m1, k)); m1 = max(m1, k); }
  sk[tid * 2] = m1; sk[tid * 2 + 1] = m2;
  __syncthreads();
  m1 = 0; m2 = 0;
  for (int i = 0; i < 512; ++i) { unsigned k = sk[i]; m2 = max(m2, min(m1, k)); m1 = max(m1, k); }
  const int sel[2] = {2047 - (int)(m1 & 2047), 2047 - (int)(m2 & 2047)};
  if (blockIdx.x == 0 && tid < 2) {
    unsigned k = tid ? m2 : m1; k &= 0xfffff800u; k = (k & 0x80000000u) ? (k & 0x7fffffffu) : ~k;
    const float r = rbf(BC(float, k));
    *(int*)(ws + WS_SEL + tid * 4) = sel[tid];
    *(float*)(ws + WS_SEL + 8 + tid * 4) = rbf(1.0f / (1.0f + __expf(-r)));
  }
  for (int i = tid; i < 1024; i += 256) {
    const int t = sel[i >> 9], k = i & 511;
    const f32x2 st = *(const f32x2*)(ws + WS_STATS + (size_t)t * 8);
    const float xv = (float)*(const __bf16*)(x + (size_t)t * 1024 + k * 2);
    xs[i >> 9][k] = rbf(rbf(xv - st[0]) / st[1]);
  }
  __syncthreads();
  const int f0 = blockIdx.x * 32, kq = tid >> 4, c = tid & 15;
  float a[8] = {0, 0, 0, 0, 0, 0, 0, 0};
  for (int kk = 0; kk < 32; ++kk) {
    const int k = kq * 32 + kk;
    const unsigned g = *(const unsigned*)(wg + ((size_t)k * F_ + f0 + c * 2) * 2), u = *(const unsigned*)(wu + ((size_t)k * F_ + f0 + c * 2) * 2);
    const float x0 = xs[0][k], x1 = xs[1][k];
    a[0] += x0 * bflo(g); a[1] += x0 * bfhi(g); a[2] += x0 * bflo(u); a[3] += x0 * bfhi(u);
    a[4] += x1 * bflo(g); a[5] += x1 * bfhi(g); a[6] += x1 * bflo(u); a[7] += x1 * bfhi(u);
  }
  for (int i = 0; i < 8; ++i) red[kq][c][i] = a[i];
  __syncthreads();
  if (tid < 64) {
    const int t = tid >> 5, f = tid & 31;
    float g = 0, u = 0;
    for (int i = 0; i < 16; ++i) { g += red[i][f >> 1][t * 4 + (f & 1)]; u += red[i][f >> 1][t * 4 + 2 + (f & 1)]; }
    g = rbf(g); u = rbf(u);
    hs[t][f] = rbf(rbf(g / (1.0f + __expf(-g))) * u);
  }
  __syncthreads();
  float y[4] = {0, 0, 0, 0};
  for (int f = 0; f < 32; ++f) {
    const unsigned wv = *(const unsigned*)(wd + ((size_t)(f0 + f) * D_ + tid * 2) * 2);
    y[0] += hs[0][f] * bflo(wv); y[1] += hs[0][f] * bfhi(wv); y[2] += hs[1][f] * bflo(wv); y[3] += hs[1][f] * bfhi(wv);
  }
  float* yp = (float*)(ws + WS_YP) + (size_t)blockIdx.x * 1024;
  *(f32x2*)(yp + tid * 2) = f32x2{y[0], y[1]};
  *(f32x2*)(yp + 512 + tid * 2) = f32x2{y[2], y[3]};
}

constexpr int REC = 2080;
constexpr int K2_EX = 64 * REC;
constexpr int K2_EX2 = K2_EX + 8192;
constexpr int K2_OB = K2_EX2 + 4096;
constexpr int K2_LDS = K2_OB + 4096;

#define EX(s, i) s[i] = __builtin_amdgcn_exp2f(s[i])
DI f32x8 lo8(const f32x16& s) { return __builtin_shufflevector(s, s, 0, 1, 2, 3, 4, 5, 6, 7); }
DI f32x8 hi8(const f32x16& s) { return __builtin_shufflevector(s, s, 8, 9, 10, 11, 12, 13, 14, 15); }

DI void att_step(f32x16& s, f32x16& sp, f32x16& o, bf16x8& kf, bf16x8& v0, bf16x8& v1, const bf16x8& q,
                 const char* kp, const char* vp0, const char* vp1, int off) {
  f32x16 zero; for (int i = 0; i < 16; ++i) zero[i] = 0.f;
  EX(s, 0); EX(s, 1); SB;
  o = SMF(v1, hi8(sp), o); SB;
  v1 = *(const bf16x8*)(vp1 + off); SB;
  EX(s, 2); EX(s, 3); EX(s, 4); EX(s, 5); EX(s, 6); SB;
  sp = MFMA32(kf, q, zero); SB;
  kf = *(const bf16x8*)(kp + off + 2 * REC); SB;
  EX(s, 7); EX(s, 8); EX(s, 9); EX(s, 10); EX(s, 11); SB;
  o = SMF(v0, lo8(s), o); SB;
  v0 = *(const bf16x8*)(vp0 + off + REC); SB;
  EX(s, 12); EX(s, 13); EX(s, 14); EX(s, 15); SB;
}

__global__ __launch_bounds__(512, 2) void k2_attn(const char* __restrict__ x, const char* __restrict__ ws, char* __restrict__ out) {
  extern __shared__ __attribute__((aligned(16))) char smem[];
  const int tid = threadIdx.x, lane = tid & 63, w = __builtin_amdgcn_readfirstlane(tid >> 6);
  const int b = blockIdx.x, tb = (b & 7) * 32 + (b >> 3), tok0 = tb * 8;
  const unsigned lds0 = (unsigned)(size_t)smem;
  const int kg = w >> 2, qt = w & 3;
  const int r = lane & 31, h = lane >> 5, n = lane & 15, g4 = lane >> 4;

  bf16x8 qf[2];
  qf[0] = *(const bf16x8*)(ws + WS_Q + (size_t)((tb * 8 + qt) * 64 + lane) * 16);
  qf[1] = *(const bf16x8*)(ws + WS_Q + (size_t)((tb * 8 + 4 + qt) * 64 + lane) * 16);
  const f32x2 knv = *(const f32x2*)(ws + WS_KN + lane * 8);
  const f32x2 vnv = *(const f32x2*)(ws + WS_KN + 512 + lane * 8);
  const int etok = tok0 + (n & 7), ecol = 64 * w + 16 * g4 + 8 * (n >> 3);
  const u32x4 xv = *(const u32x4*)(x + (size_t)etok * 1024 + ecol * 2);
  const f32x2 stv = *(const f32x2*)(ws + WS_STATS + (size_t)etok * 8);
  const int sel0 = *(const int*)(ws + WS_SEL), sel1 = *(const int*)(ws + WS_SEL + 4);
#pragma unroll
  for (int c = 0; c < 4; ++c) {
#pragma unroll
    for (int i = 0; i < 4; ++i) {
      const int rec = kg * 32 + 8 * c + qt * 2 + (i >> 1);
      glds16(ws + WS_KV + (size_t)rec * 2048 + (i & 1) * 1024 + lane * 16, lds0 + rec * REC + (i & 1) * 1024);
    }
  }
  if (tid < 128) {
    const unsigned v = (tid & 1) ? 0u : 0x3f803f80u;
    *(u32x4*)(smem + (tid >> 1) * REC + 2048 + (tid & 1) * 16) = u32x4{v, v, v, v};
  }
  const char* kp0 = smem + kg * 32 * REC + lane * 16;
  const int voff = (r < 16) ? (h * 16 + r) * 16 : 0;
  const int coff = (r == 16 || r == 20) ? 2048 : 2064;
  const char* vp00 = smem + kg * 32 * REC + ((r < 16) ? 1024 + voff : coff);
  const char* vp10 = smem + kg * 32 * REC + ((r < 16) ? 1536 + voff : coff);

  VMWAIT(12);
  const float kn = wavemax(fmaxf(knv[0], knv[1])), vn = wavemax(fmaxf(vnv[0], vnv[1]));
  bool fast[2];
#pragma unroll
  for (int rd = 0; rd < 2; ++rd) {
    const u32x4 qu = BC(u32x4, qf[rd]);
    float n2 = 0.f;
#pragma unroll
    for (int e = 0; e < 4; ++e) n2 = dot2(qu[e], qu[e], n2);
    n2 = wavemax(halfsum(n2));
    const bool ok = (n2 * kn <= 2200.0f) && (vn <= 1.0e12f) && (vn >= 1.0e-12f);
    fast[rd] = __builtin_amdgcn_readfirstlane((int)ok) != 0;
  }
  f32x4 sacc[4];
#pragma unroll
  for (int i = 0; i < 4; ++i) sacc[i] = f32x4{0.f, 0.f, 0.f, 0.f};
  bf16x8 wof[32];
  auto load_wo = [&](int rd) {
    const char* base = ws + WS_WO + (size_t)(w * 2 + rd) * 32768;
    const unsigned lo = lane * 16;
#pragma unroll
    for (int i = 0; i < 32; ++i) wof[i] = *(const bf16x8*)(base + i * 1024 + lo);
    asm volatile("" ::: "memory");
  };
  f32x16 zero; for (int i = 0; i < 16; ++i) zero[i] = 0.f;

#pragma unroll
  for (int rd = 0; rd < 2; ++rd) {
    const bf16x8 q = qf[rd];
    f32x16 o = zero;
    float mrow = 0.f;
    if (rd == 0) BARRIER();
    if (fast[rd]) {
      const char *kp = kp0, *vp0 = vp00, *vp1 = vp10;
      bf16x8 kf = *(const bf16x8*)(kp);
      f32x16 sa = MFMA32(kf, q, zero), sb = zero;
      kf = *(const bf16x8*)(kp + REC);
      bf16x8 v0 = *(const bf16x8*)(vp0), v1 = v0;
      auto group = [&]() {
        att_step(sa, sb, o, kf, v0, v1, q, kp, vp0, vp1, 0);
        att_step(sb, sa, o, kf, v0, v1, q, kp, vp0, vp1, REC);
        att_step(sa, sb, o, kf, v0, v1, q, kp, vp0, vp1, 2 * REC);
        att_step(sb, sa, o, kf, v0, v1, q, kp, vp0, vp1, 3 * REC);
        kp += 4 * REC; vp0 += 4 * REC; vp1 += 4 * REC;
      };
      if (rd == 0) {
        group();
        VMWAIT(8); BARRIER();
        group();
        VMWAIT(0); BARRIER();
      }
      load_wo(rd);
      for (int t = (rd == 0 ? 2 : 0); t < 8; ++t) group();
      o = SMF(v1, hi8(sb), o);
    } else {
      if (rd == 0) { VMWAIT(8); BARRIER(); VMWAIT(0); BARRIER(); }
      load_wo(rd);
      mrow = -3.0e38f;
      for (int t = 0; t < 32; ++t) {
        const bf16x8 kf = *(const bf16x8*)(kp0 + t * REC);
        const bf16x8 v0 = *(const bf16x8*)(vp00 + t * REC), v1 = *(const bf16x8*)(vp10 + t * REC);
        f32x16 s = MFMA32(kf, q, zero);
        float mx = s[0];
#pragma unroll
        for (int i = 1; i < 16; ++i) mx = fmaxf(mx, s[i]);
        mx = fmaxf(halfmax(mx), mrow);
        const float alpha = __builtin_amdgcn_exp2f(mrow - mx);
        mrow = mx;
#pragma unroll
        for (int i = 0; i < 9; ++i) o[i] *= alpha;
#pragma unroll
        for (int i = 0; i < 16; ++i) s[i] = __builtin_amdgcn_exp2f(s[i] - mx);
        o = SMF(v0, lo8(s), o);
        o = SMF(v1, hi8(s), o);
      }
    }
    {
      const f32x4 lo = {o[0], o[1], o[2], o[3]}, hi = {o[4], o[5], o[6], o[7]};
      const f32x4 send = kg ? lo : hi, keep = kg ? hi : lo;
      *(f32x4*)(smem + K2_EX + w * 1024 + lane * 16) = send;
      *(f32x2*)(smem + K2_EX2 + w * 512 + lane * 8) = f32x2{o[8], mrow};
      BARRIER();
      const f32x4 other = *(const f32x4*)(smem + K2_EX + (w ^ 4) * 1024 + lane * 16);
      const f32x2 lm = *(const f32x2*)(smem + K2_EX2 + (w ^ 4) * 512 + lane * 8);
      const float mt = fmaxf(mrow, lm[1]);
      const float fa = __builtin_amdgcn_exp2f(mrow - mt), fb = __builtin_amdgcn_exp2f(lm[1] - mt);
      const float il = 1.0f / (o[8] * fa + lm[0] * fb);
      const float ca = fa * il, cb2 = fb * il;
      const u32x2 ov = {pk2(keep[0] * ca + other[0] * cb2, keep[1] * ca + other[1] * cb2), pk2(keep[2] * ca + other[2] * cb2, keep[3] * ca + other[3] * cb2)};
      *(u32x2*)(smem + K2_OB + ((qt * 4 + (r >> 3)) * 2 + h) * 128 + (r & 7) * 16 + kg * 8) = ov;
      BARRIER();
#pragma unroll
      for (int ks = 0; ks < 8; ++ks) {
        const bf16x8 of = *(const bf16x8*)(smem + K2_OB + ks * 512 + g4 * 128 + (n & 7) * 16);
#pragma unroll
        for (int i = 0; i < 4; ++i) sacc[i] = MFMA16(wof[ks * 4 + i], of, sacc[i]);
      }
    }
  }
  float gv[8], xf[8];
  {
    const float inv = 1.0f / stv[1];
#pragma unroll
    for (int e = 0; e < 8; ++e) {
      xf[e] = (e & 1) ? bfhi(xv[e >> 1]) : bflo(xv[e >> 1]);
      gv[e] = rbf(rbf(xf[e] - stv[0]) * inv);
    }
  }
  if (etok == sel0 || etok == sel1) {
    const int which = (etok == sel0) ? 0 : 1;
    const float rs = *(const float*)(ws + WS_SEL + 8 + which * 4);
    const float* yp = (const float*)(ws + WS_YP) + which * 512 + ecol;
    f32x4 y0 = {0.f, 0.f, 0.f, 0.f}, y1 = y0;
    for (int i = 0; i < 56; ++i) { y0 += *(const f32x4*)(yp + i * 1024); y1 += *(const f32x4*)(yp + i * 1024 + 4); }
#pragma unroll
    for (int e = 0; e < 8; ++e) gv[e] = rbf(gv[e] + rbf(rbf(e < 4 ? y0[e & 3] : y1[e & 3]) * rs));
  }
  float ov[8];
#pragma unroll
  for (int e = 0; e < 8; ++e) {
    const float sv = (n < 8) ? sacc[e >> 2][e & 3] : sacc[2 + (e >> 2)][e & 3];
    const float gg = rbf(gv[e] / (1.0f + __builtin_amdgcn_exp2f(-1.4426950408889634f * gv[e])));
    ov[e] = xf[e] + rbf(rbf(sv) * gg);
  }
  *(u32x4*)(out + (size_t)etok * 1024 + ecol * 2) = u32x4{pk2(ov[0], ov[1]), pk2(ov[2], ov[3]), pk2(ov[4], ov[5]), pk2(ov[6], ov[7])};
}

extern "C" void kernel_launch(void* const* d_in, const int* in_sizes, int n_in, void* d_out, int out_size, void* d_ws, size_t ws_size,
                              hipStream_t stream) {
  const char* x = (const char*)d_in[0];
  char* ws = (char*)d_ws;
  hipFuncSetAttribute((const void*)k1_proj, hipFuncAttributeMaxDynamicSharedMemorySize, K1_LDS);
  hipFuncSetAttribute((const void*)k2_attn, hipFuncAttributeMaxDynamicSharedMemorySize, K2_LDS);
  k1_proj<<<256, 512, K1_LDS, stream>>>(x, (const char*)d_in[1], (const char*)d_in[2], (const char*)d_in[3], (const char*)d_in[4],
                                        (const char*)d_in[6], ws);
  kf_ffn<<<56, 256, 0, stream>>>(x, (const char*)d_in[7], (const char*)d_in[8], (const char*)d_in[9], ws);
  k2_attn<<<256, 512, K2_LDS, stream>>>(x, ws, (char*)d_out);
}
```

```cpp
#include <hip/hip_runtime.h>
#include <hip/hip_bf16.h>
#include <stdint.h>

typedef __attribute__((ext_vector_type(16))) __bf16 bf16x16;
typedef __attribute__((ext_vector_type(8))) __bf16 bf16x8;
typedef __attribute__((ext_vector_type(4))) __bf16 bf16x4;
typedef __attribute__((ext_vector_type(2))) __bf16 bf16x2;
typedef __attribute__((ext_vector_type(16))) float f32x16;
typedef __attribute__((ext_vector_type(8))) float f32x8;
typedef __attribute__((ext_vector_type(4))) float f32x4;
typedef __attribute__((ext_vector_type(2))) float f32x2;
typedef __attribute__((ext_vector_type(4))) unsigned u32x4;
typedef __attribute__((ext_vector_type(2))) unsigned u32x2;
#define LAS __attribute__((address_space(3)))
#define GAS __attribute__((address_space(1)))
typedef GAS unsigned long long gu64;
typedef GAS unsigned gu32;
#define RLX_AGENT __ATOMIC_RELAXED, __HIP_MEMORY_SCOPE_AGENT
#define DI __device__ __forceinline__
#define BC(T, v) __builtin_bit_cast(T, v)
#define MFMA32(a, b, c) __builtin_amdgcn_mfma_f32_32x32x16_bf16((a), (b), (c), 0, 0, 0)
#define MFMA16(a, b, c) __builtin_amdgcn_mfma_f32_16x16x32_bf16((a), (b), (c), 0, 0, 0)
#define SMF(a, b8, c) __builtin_amdgcn_smfmac_f32_32x32x32_bf16((a), BC(bf16x16, b8), (c), (int)0xDDDDDDDD, 0, 0)
#define SB __builtin_amdgcn_sched_barrier(0)

constexpr int S_ = 2048, D_ = 512, F_ = 1792;
constexpr size_t WS_EPOCH = 0;
constexpr size_t WS_KVF = 5u << 20;
constexpr size_t WS_PG = 4608u << 10;
constexpr size_t WS_KEYG = 16384;
constexpr size_t WS_STG = WS_KEYG + 4096;
constexpr size_t WS_HG = WS_STG + 16384;
constexpr size_t WS_YG = WS_HG + 16384;
constexpr size_t WS_KV = 131072;
constexpr size_t WS_OX = 262144;

DI float rbf(float x) { return (float)(__bf16)x; }
DI unsigned pk2(float a, float b) { f32x2 v = {a, b}; return BC(unsigned, __builtin_convertvector(v, bf16x2)); }
DI float bflo(unsigned u) { return BC(float, u << 16); }
DI float bfhi(unsigned u) { return BC(float, u & 0xffff0000u); }
DI float dot2(unsigned a, unsigned b, float c) { return __builtin_amdgcn_fdot2_f32_bf16(BC(bf16x2, a), BC(bf16x2, b), c, false); }
DI float halfsum(float v) { auto r = __builtin_amdgcn_permlane32_swap(BC(unsigned, v), BC(unsigned, v), false, false); const unsigned a = r[0], b = r[1]; return BC(float, a) + BC(float, b); }
DI float halfmax(float v) { auto r = __builtin_amdgcn_permlane32_swap(BC(unsigned, v), BC(unsigned, v), false, false); const unsigned a = r[0], b = r[1]; return fmaxf(BC(float, a), BC(float, b)); }
template <int CTRL, int RM = 0xf> DI float dppf(float x) { return BC(float, __builtin_amdgcn_update_dpp(0, BC(int, x), CTRL, RM, 0xf, true)); }
DI float rowsum16(float x) { x += dppf<0xB1>(x); x += dppf<0x4E>(x); x += dppf<0x141>(x); x += dppf<0x140>(x); return x; }
DI float rowmax16(float x) { x = fmaxf(x, dppf<0xB1>(x)); x = fmaxf(x, dppf<0x4E>(x)); x = fmaxf(x, dppf<0x141>(x)); x = fmaxf(x, dppf<0x140>(x)); return x; }
DI float x16sum(float v) { auto r = __builtin_amdgcn_permlane16_swap(BC(unsigned, v), BC(unsigned, v), false, false); const unsigned a = r[0], b = r[1]; return BC(float, a) + BC(float, b); }
DI float x16max(float v) { auto r = __builtin_amdgcn_permlane16_swap(BC(unsigned, v), BC(unsigned, v), false, false); const unsigned a = r[0], b = r[1]; return fmaxf(BC(float, a), BC(float, b)); }
template <int CTRL> DI unsigned dppu(unsigned x) { return (unsigned)__builtin_amdgcn_update_dpp(0, (int)x, CTRL, 0xf, 0xf, true); }
DI unsigned umax8(unsigned x) { x = max(x, dppu<0xB1>(x)); x = max(x, dppu<0x4E>(x)); x = max(x, dppu<0x141>(x)); return x; }
DI unsigned wavemaxu(unsigned x) {
  x = umax8(x); x = max(x, dppu<0x140>(x));
  { auto r = __builtin_amdgcn_permlane16_swap(x, x, false, false); const unsigned a = r[0], b = r[1]; x = max(a, b); }
  { auto r = __builtin_amdgcn_permlane32_swap(x, x, false, false); const unsigned a = r[0], b = r[1]; x = max(a, b); }
  return x;
}
DI float wavesum(float x) { return halfsum(x16sum(rowsum16(x))); }
DI float wavemax(float x) { return halfmax(x16max(rowmax16(x))); }

DI void glds16(const void* gsrc, unsigned lds_dst) {
  unsigned keep;
  asm volatile("s_mov_b32 %0, m0\n\ts_mov_b32 m0, %2\n\ts_nop 0\n\tglobal_load_lds_dwordx4 %1, off\n\ts_mov_b32 m0, %0"
               : "=&s"(keep) : "v"(gsrc), "s"(lds_dst) : "memory");
}
DI void glds16s(const void* sbase, unsigned voff, unsigned lds_dst) {
  unsigned keep;
  asm volatile("s_mov_b32 %0, m0\n\ts_mov_b32 m0, %3\n\ts_nop 0\n\tglobal_load_lds_dwordx4 %1, %2\n\ts_mov_b32 m0, %0"
               : "=&s"(keep) : "v"(voff), "s"(sbase), "s"(lds_dst) : "memory");
}
DI void glds4c(const void* gsrc, unsigned lds_dst) {
  unsigned keep;
  asm volatile("s_mov_b32 %0, m0\n\ts_mov_b32 m0, %2\n\ts_nop 0\n\tglobal_load_lds_dword %1, off sc1\n\ts_mov_b32 m0, %0"
               : "=&s"(keep) : "v"(gsrc), "s"(lds_dst) : "memory");
}
DI void glds4(const void* gsrc, unsigned lds_dst) {
  unsigned keep;
  asm volatile("s_mov_b32 %0, m0\n\ts_mov_b32 m0, %2\n\ts_nop 0\n\tglobal_load_lds_dword %1, off\n\ts_mov_b32 m0, %0"
               : "=&s"(keep) : "v"(gsrc), "s"(lds_dst) : "memory");
}
#define VMWAIT_(n) asm volatile("s_waitcnt vmcnt(" #n ")" ::: "memory")
#define VMWAIT(n) VMWAIT_(n)
#define BARRIER() do { asm volatile("s_waitcnt lgkmcnt(0)" ::: "memory"); __builtin_amdgcn_s_barrier(); asm volatile("" ::: "memory"); } while (0)

DI bf16x4 trread(const char* p) { return __builtin_amdgcn_ds_read_tr16_b64_v4bf16((LAS bf16x4*)p); }
DI bf16x8 cat(bf16x4 a, bf16x4 b) { return __builtin_shufflevector(a, b, 0, 1, 2, 3, 4, 5, 6, 7); }

DI void ln_stats(float s1, float s2, float& m, float& sq) {
  float mean = s1 * (1.0f / 512.0f);
  float var = fmaxf(s2 * (1.0f / 512.0f) - mean * mean, 0.0f);
  m = rbf(mean);
  float ve = rbf(rbf(var) + 1e-5f);
  sq = rbf(__builtin_amdgcn_sqrtf(ve));
}

template <int N> struct IC { static constexpr int value = N; };
template <int B, int E, class F> DI void static_for(F&& f) { if constexpr (B < E) { f(IC<B>{}); static_for<B + 1, E>(f); } }
#define COMPUTE_ASM \
  "s_mov_b64 s[40:41], %0\n" \
  "v_mbcnt_lo_u32_b32 v61, -1, 0\n" \
  "v_mbcnt_hi_u32_b32 v61, -1, v61\n" \
  "v_lshlrev_b32 v57, 4, v61\n" \
  "v_add_u32 v63, %4, v57\n" \
  "v_and_b32 v62, 31, v61\n" \
  "v_lshrrev_b32 v58, 5, v61\n" \
  "v_lshl_add_u32 v58, v58, 4, v62\n" \
  "v_lshlrev_b32 v58, 4, v58\n" \
  "s_mov_b32 s44, 0xffff\n" \
  "s_mov_b32 s45, 0xffff\n" \
  "v_and_b32 v62, 27, v62\n" \
  "v_cmp_eq_u32 vcc, 16, v62\n" \
  "v_mov_b32 v60, 0x3f803f80\n" \
  "v_mov_b32 v56, 0xdddddddd\n" \
  "v_cndmask_b32 v60, 0, v60, vcc\n" \
  "v_add_u32 v59, %3, v57\n" \
  "v_mov_b32 v61, v60\n" \
  "v_mov_b32 v16, %6\n" \
  "v_and_b32 v18, 0xf0, v57\n" \
  "v_lshlrev_b32 v18, 2, v18\n" \
  "v_mov_b32 v32, 0\n" \
  "v_xor_b32 v32, %2, v56\n" \
  "ds_read_b32 v17, v16\n" \
  "s_mov_b32 s48, 0x40000\n" \
  "s_waitcnt lgkmcnt(0)\n" \
  "v_cmp_eq_u32 s[46:47], 0, v17\n" \
  "s_cmp_lg_u32 s46, 0\n" \
  "s_cbranch_scc1 Lcw_c%=\n" \
  "global_load_dwordx4 v[32:35], v18, %1 sc1\n" \
  "Lcw_c%=:\n" \
  "v_mov_b64 v[128:129], v[60:61]\n" \
  "v_mov_b64 v[130:131], v[60:61]\n" \
  "v_mov_b64 v[132:133], v[60:61]\n" \
  "v_mov_b64 v[134:135], v[60:61]\n" \
  "v_mov_b64 v[136:137], v[60:61]\n" \
  "v_mov_b64 v[138:139], v[60:61]\n" \
  "v_mov_b64 v[140:141], v[60:61]\n" \
  "v_mov_b64 v[142:143], v[60:61]\n" \
  "v_mov_b64 v[144:145], v[60:61]\n" \
  "v_mov_b64 v[146:147], v[60:61]\n" \
  "v_mov_b64 v[148:149], v[60:61]\n" \
  "v_mov_b64 v[150:151], v[60:61]\n" \
  "v_mov_b64 v[152:153], v[60:61]\n" \
  "v_mov_b64 v[154:155], v[60:61]\n" \
  "v_mov_b64 v[156:157], v[60:61]\n" \
  "v_mov_b64 v[158:159], v[60:61]\n" \
  "v_mov_b64 v[160:161], v[60:61]\n" \
  "v_mov_b64 v[162:163], v[60:61]\n" \
  "v_mov_b64 v[164:165], v[60:61]\n" \
  "v_mov_b64 v[166:167], v[60:61]\n" \
  "v_mov_b64 v[168:169], v[60:61]\n" \
  "v_mov_b64 v[170:171], v[60:61]\n" \
  "v_mov_b64 v[172:173], v[60:61]\n" \
  "v_mov_b64 v[174:175], v[60:61]\n" \
  "v_mov_b64 v[176:177], v[60:61]\n" \
  "v_mov_b64 v[178:179], v[60:61]\n" \
  "v_mov_b64 v[180:181], v[60:61]\n" \
  "v_mov_b64 v[182:183], v[60:61]\n" \
  "v_mov_b64 v[184:185], v[60:61]\n" \
  "v_mov_b64 v[186:187], v[60:61]\n" \
  "v_mov_b64 v[188:189], v[60:61]\n" \
  "v_mov_b64 v[190:191], v[60:61]\n" \
  "v_mov_b64 v[192:193], v[60:61]\n" \
  "v_mov_b64 v[194:195], v[60:61]\n" \
  "v_mov_b64 v[196:197], v[60:61]\n" \
  "v_mov_b64 v[198:199], v[60:61]\n" \
  "v_mov_b64 v[200:201], v[60:61]\n" \
  "v_mov_b64 v[202:203], v[60:61]\n" \
  "v_mov_b64 v[204:205], v[60:61]\n" \
  "v_mov_b64 v[206:207], v[60:61]\n" \
  "v_mov_b64 v[208:209], v[60:61]\n" \
  "v_mov_b64 v[210:211], v[60:61]\n" \
  "v_mov_b64 v[212:213], v[60:61]\n" \
  "v_mov_b64 v[214:215], v[60:61]\n" \
  "v_mov_b64 v[216:217], v[60:61]\n" \
  "v_mov_b64 v[218:219], v[60:61]\n" \
  "v_mov_b64 v[220:221], v[60:61]\n" \
  "v_mov_b64 v[222:223], v[60:61]\n" \
  "v_mov_b64 v[224:225], v[60:61]\n" \
  "v_mov_b64 v[226:227], v[60:61]\n" \
  "v_mov_b64 v[228:229], v[60:61]\n" \
  "v_mov_b64 v[230:231], v[60:61]\n" \
  "v_mov_b64 v[232:233], v[60:61]\n" \
  "v_mov_b64 v[234:235], v[60:61]\n" \
  "v_mov_b64 v[236:237], v[60:61]\n" \
  "v_mov_b64 v[238:239], v[60:61]\n" \
  "v_mov_b64 v[240:241], v[60:61]\n" \
  "v_mov_b64 v[242:243], v[60:61]\n" \
  "v_mov_b64 v[244:245], v[60:61]\n" \
  "v_mov_b64 v[246:247], v[60:61]\n" \
  "v_mov_b64 v[248:249], v[60:61]\n" \
  "v_mov_b64 v[250:251], v[60:61]\n" \
  "v_mov_b64 v[252:253], v[60:61]\n" \
  "v_mov_b64 v[254:255], v[60:61]\n" \
  "v_mov_b64 v[0:1], 0\n" \
  "v_mov_b64 v[2:3], 0\n" \
  "v_mov_b64 v[4:5], 0\n" \
  "v_mov_b64 v[6:7], 0\n" \
  "v_mov_b64 v[8:9], 0\n" \
  "v_mov_b64 v[10:11], 0\n" \
  "v_mov_b64 v[12:13], 0\n" \
  "v_mov_b64 v[14:15], 0\n" \
  "v_mov_b32 v62, %5\n" \
  "s_cmp_eq_u32 s46, 0\n" \
  "s_cbranch_scc1 Lcw_chk%=\n" \
  "Lcw_pub%=:\n" \
  "ds_read_b32 v17, v16\n" \
  "s_waitcnt lgkmcnt(0)\n" \
  "v_cmp_eq_u32 vcc, 0, v17\n" \
  "s_cbranch_vccz Lcw_pub2%=\n" \
  "s_sleep 1\n" \
  "s_branch Lcw_pub%=\n" \
  "Lcw_pub2%=:\n" \
  "s_sleep 10\n" \
  "Lcw_poll%=:\n" \
  "global_load_dwordx4 v[32:35], v18, %1 sc1\n" \
  "Lcw_chk%=:\n" \
  "s_waitcnt vmcnt(0)\n" \
  "v_xor_b32 v32, %2, v32\n" \
  "v_xor_b32 v33, %2, v33\n" \
  "v_xor_b32 v34, %2, v34\n" \
  "v_xor_b32 v35, %2, v35\n" \
  "v_or3_b32 v32, v32, v33, v34\n" \
  "v_or_b32 v32, v32, v35\n" \
  "v_cmp_ne_u32 vcc, 0, v32\n" \
  "s_cbranch_vccz Lcw_go%=\n" \
  "s_sub_u32 s48, s48, 1\n" \
  "s_cmp_lg_u32 s48, 0\n" \
  "s_cbranch_scc1 Lcw_poll%=\n" \
  "Lcw_go%=:\n" \
  "global_load_dwordx4 v[64:67], v57, s[40:41] offset:0 sc1\n" \
  "global_load_dwordx4 v[68:71], v57, s[40:41] offset:2048 sc1\n" \
  "s_mov_b64 exec, s[44:45]\n" \
  "global_load_dwordx4 v[128:131], v58, s[40:41] offset:1024 sc1\n" \
  "global_load_dwordx4 v[132:135], v58, s[40:41] offset:1536 sc1\n" \
  "s_mov_b64 exec, -1\n" \
  "s_mov_b64 exec, s[44:45]\n" \
  "global_load_dwordx4 v[136:139], v58, s[40:41] offset:3072 sc1\n" \
  "global_load_dwordx4 v[140:143], v58, s[40:41] offset:3584 sc1\n" \
  "s_mov_b64 exec, -1\n" \
  "s_add_u32 s40, s40, 4096\n" \
  "s_addc_u32 s41, s41, 0\n" \
  "global_load_dwordx4 v[72:75], v57, s[40:41] offset:0 sc1\n" \
  "global_load_dwordx4 v[76:79], v57, s[40:41] offset:2048 sc1\n" \
  "s_mov_b64 exec, s[44:45]\n" \
  "global_load_dwordx4 v[144:147], v58, s[40:41] offset:1024 sc1\n" \
  "global_load_dwordx4 v[148:151], v58, s[40:41] offset:1536 sc1\n" \
  "s_mov_b64 exec, -1\n" \
  "s_mov_b64 exec, s[44:45]\n" \
  "global_load_dwordx4 v[152:155], v58, s[40:41] offset:3072 sc1\n" \
  "global_load_dwordx4 v[156:159], v58, s[40:41] offset:3584 sc1\n" \
  "s_mov_b64 exec, -1\n" \
  "Lcw_q%=:\n" \
  "ds_read_b32 v17, v16 offset:32\n" \
  "s_waitcnt lgkmcnt(0)\n" \
  "v_cmp_eq_u32 vcc, 0, v17\n" \
  "s_cbranch_vccnz Lcw_q%=\n" \
  "ds_read_b128 v[48:51], v63\n" \
  "ds_read_b128 v[52:55], v63 offset:1024\n" \
  "s_waitcnt vmcnt(11)\n" \
  "s_waitcnt lgkmcnt(0)\n" \
  "v_mfma_f32_32x32x16_bf16 v[16:31], v[64:67], v[48:51], 0\n" \
  "s_nop 7\n" \
  "s_nop 7\n" \
  "v_exp_f32 v16, v16\n" \
  "v_exp_f32 v17, v17\n" \
  "s_add_u32 s40, s40, 4096\n" \
  "s_addc_u32 s41, s41, 0\n" \
  "global_load_dwordx4 v[80:83], v57, s[40:41] offset:0 sc1\n" \
  "global_load_dwordx4 v[84:87], v57, s[40:41] offset:2048 sc1\n" \
  "v_exp_f32 v18, v18\n" \
  "v_exp_f32 v19, v19\n" \
  "v_exp_f32 v20, v20\n" \
  "s_waitcnt vmcnt(12)\n" \
  "v_mfma_f32_32x32x16_bf16 v[32:47], v[68:71], v[48:51], 0\n" \
  "s_mov_b64 exec, s[44:45]\n" \
  "global_load_dwordx4 v[160:163], v58, s[40:41] offset:1024 sc1\n" \
  "global_load_dwordx4 v[164:167], v58, s[40:41] offset:1536 sc1\n" \
  "s_mov_b64 exec, -1\n" \
  "v_exp_f32 v21, v21\n" \
  "v_exp_f32 v22, v22\n" \
  "v_exp_f32 v23, v23\n" \
  "v_exp_f32 v24, v24\n" \
  "v_exp_f32 v25, v25\n" \
  "s_waitcnt vmcnt(13)\n" \
  "v_smfmac_f32_32x32x32_bf16 v[0:15], v[128:131], v[16:23], v56\n" \
  "s_mov_b64 exec, s[44:45]\n" \
  "global_load_dwordx4 v[168:171], v58, s[40:41] offset:3072 sc1\n" \
  "global_load_dwordx4 v[172:175], v58, s[40:41] offset:3584 sc1\n" \
  "s_mov_b64 exec, -1\n" \
  "v_exp_f32 v26, v26\n" \
  "v_exp_f32 v27, v27\n" \
  "v_exp_f32 v28, v28\n" \
  "v_exp_f32 v29, v29\n" \
  "v_exp_f32 v30, v30\n" \
  "v_exp_f32 v31, v31\n" \
  "v_exp_f32 v32, v32\n" \
  "v_exp_f32 v33, v33\n" \
  "s_waitcnt vmcnt(14)\n" \
  "v_smfmac_f32_32x32x32_bf16 v[0:15], v[132:135], v[24:31], v56\n" \
  "s_add_u32 s40, s40, 4096\n" \
  "s_addc_u32 s41, s41, 0\n" \
  "global_load_dwordx4 v[88:91], v57, s[40:41] offset:0 sc1\n" \
  "global_load_dwordx4 v[92:95], v57, s[40:41] offset:2048 sc1\n" \
  "v_exp_f32 v34, v34\n" \
  "v_exp_f32 v35, v35\n" \
  "v_exp_f32 v36, v36\n" \
  "s_waitcnt vmcnt(13)\n" \
  "v_mfma_f32_32x32x16_bf16 v[16:31], v[72:75], v[48:51], 0\n" \
  "s_mov_b64 exec, s[44:45]\n" \
  "global_load_dwordx4 v[176:179], v58, s[40:41] offset:1024 sc1\n" \
  "global_load_dwordx4 v[180:183], v58, s[40:41] offset:1536 sc1\n" \
  "s_mov_b64 exec, -1\n" \
  "v_exp_f32 v37, v37\n" \
  "v_exp_f32 v38, v38\n" \
  "v_exp_f32 v39, v39\n" \
  "v_exp_f32 v40, v40\n" \
  "v_exp_f32 v41, v41\n" \
  "v_smfmac_f32_32x32x32_bf16 v[0:15], v[136:139], v[32:39], v56\n" \
  "s_mov_b64 exec, s[44:45]\n" \
  "global_load_dwordx4 v[184:187], v58, s[40:41] offset:3072 sc1\n" \
  "global_load_dwordx4 v[188:191], v58, s[40:41] offset:3584 sc1\n" \
  "s_mov_b64 exec, -1\n" \
  "v_exp_f32 v42, v42\n" \
  "v_exp_f32 v43, v43\n" \
  "v_exp_f32 v44, v44\n" \
  "v_exp_f32 v45, v45\n" \
  "v_exp_f32 v46, v46\n" \
  "v_exp_f32 v47, v47\n" \
  "v_exp_f32 v16, v16\n" \
  "v_exp_f32 v17, v17\n" \
  "v_smfmac_f32_32x32x32_bf16 v[0:15], v[140:143], v[40:47], v56\n" \
  "s_add_u32 s40, s40, 4096\n" \
  "s_addc_u32 s41, s41, 0\n" \
  "global_load_dwordx4 v[96:99], v57, s[40:41] offset:0 sc1\n" \
  "global_load_dwordx4 v[100:103], v57, s[40:41] offset:2048 sc1\n" \
  "v_exp_f32 v18, v18\n" \
  "v_exp_f32 v19, v19\n" \
  "v_exp_f32 v20, v20\n" \
  "s_waitcnt vmcnt(18)\n" \
  "v_mfma_f32_32x32x16_bf16 v[32:47], v[76:79], v[48:51], 0\n" \
  "s_mov_b64 exec, s[44:45]\n" \
  "global_load_dwordx4 v[192:195], v58, s[40:41] offset:1024 sc1\n" \
  "global_load_dwordx4 v[196:199], v58, s[40:41] offset:1536 sc1\n" \
  "s_mov_b64 exec, -1\n" \
  "v_exp_f32 v21, v21\n" \
  "v_exp_f32 v22, v22\n" \
  "v_exp_f32 v23, v23\n" \
  "v_exp_f32 v24, v24\n" \
  "v_exp_f32 v25, v25\n" \
  "s_waitcnt vmcnt(19)\n" \
  "v_smfmac_f32_32x32x32_bf16 v[0:15], v[144:147], v[16:23], v56\n" \
  "s_mov_b64 exec, s[44:45]\n" \
  "global_load_dwordx4 v[200:203], v58, s[40:41] offset:3072 sc1\n" \
  "global_load_dwordx4 v[204:207], v58, s[40:41] offset:3584 sc1\n" \
  "s_mov_b64 exec, -1\n" \
  "v_exp_f32 v26, v26\n" \
  "v_exp_f32 v27, v27\n" \
  "v_exp_f32 v28, v28\n" \
  "v_exp_f32 v29, v29\n" \
  "v_exp_f32 v30, v30\n" \
  "v_exp_f32 v31, v31\n" \
  "v_exp_f32 v32, v32\n" \
  "v_exp_f32 v33, v33\n" \
  "s_waitcnt vmcnt(20)\n" \
  "v_smfmac_f32_32x32x32_bf16 v[0:15], v[148:151], v[24:31], v56\n" \
  "s_add_u32 s40, s40, 4096\n" \
  "s_addc_u32 s41, s41, 0\n" \
  "global_load_dwordx4 v[104:107], v57, s[40:41] offset:0 sc1\n" \
  "global_load_dwordx4 v[108:111], v57, s[40:41] offset:2048 sc1\n" \
  "v_exp_f32 v34, v34\n" \
  "v_exp_f32 v35, v35\n" \
  "v_exp_f32 v36, v36\n" \
  "s_waitcnt vmcnt(19)\n" \
  "v_mfma_f32_32x32x16_bf16 v[16:31], v[80:83], v[48:51], 0\n" \
  "s_mov_b64 exec, s[44:45]\n" \
  "global_load_dwordx4 v[208:211], v58, s[40:41] offset:1024 sc1\n" \
  "global_load_dwordx4 v[212:215], v58, s[40:41] offset:1536 sc1\n" \
  "s_mov_b64 exec, -1\n" \
  "v_exp_f32 v37, v37\n" \
  "v_exp_f32 v38, v38\n" \
  "v_exp_f32 v39, v39\n" \
  "v_exp_f32 v40, v40\n" \
  "v_exp_f32 v41, v41\n" \
  "v_smfmac_f32_32x32x32_bf16 v[0:15], v[152:155], v[32:39], v56\n" \
  "s_mov_b64 exec, s[44:45]\n" \
  "global_load_dwordx4 v[216:219], v58, s[40:41] offset:3072 sc1\n" \
  "global_load_dwordx4 v[220:223], v58, s[40:41] offset:3584 sc1\n" \
  "s_mov_b64 exec, -1\n" \
  "v_exp_f32 v42, v42\n" \
  "v_exp_f32 v43, v43\n" \
  "v_exp_f32 v44, v44\n" \
  "v_exp_f32 v45, v45\n" \
  "v_exp_f32 v46, v46\n" \
  "v_exp_f32 v47, v47\n" \
  "v_exp_f32 v16, v16\n" \
  "v_exp_f32 v17, v17\n" \
  "v_smfmac_f32_32x32x32_bf16 v[0:15], v[156:159], v[40:47], v56\n" \
  "s_add_u32 s40, s40, 4096\n" \
  "s_addc_u32 s41, s41, 0\n" \
  "global_load_dwordx4 v[112:115], v57, s[40:41] offset:0 sc1\n" \
  "global_load_dwordx4 v[116:119], v57, s[40:41] offset:2048 sc1\n" \
  "v_exp_f32 v18, v18\n" \
  "v_exp_f32 v19, v19\n" \
  "v_exp_f32 v20, v20\n" \
  "s_waitcnt vmcnt(24)\n" \
  "v_mfma_f32_32x32x16_bf16 v[32:47], v[84:87], v[48:51], 0\n" \
  "s_mov_b64 exec, s[44:45]\n" \
  "global_load_dwordx4 v[224:227], v58, s[40:41] offset:1024 sc1\n" \
  "global_load_dwordx4 v[228:231], v58, s[40:41] offset:1536 sc1\n" \
  "s_mov_b64 exec, -1\n" \
  "v_exp_f32 v21, v21\n" \
  "v_exp_f32 v22, v22\n" \
  "v_exp_f32 v23, v23\n" \
  "v_exp_f32 v24, v24\n" \
  "v_exp_f32 v25, v25\n" \
  "s_waitcnt vmcnt(25)\n" \
  "v_smfmac_f32_32x32x32_bf16 v[0:15], v[160:163], v[16:23], v56\n" \
  "s_mov_b64 exec, s[44:45]\n" \
  "global_load_dwordx4 v[232:235], v58, s[40:41] offset:3072 sc1\n" \
  "global_load_dwordx4 v[236:239], v58, s[40:41] offset:3584 sc1\n" \
  "s_mov_b64 exec, -1\n" \
  "v_exp_f32 v26, v26\n" \
  "v_exp_f32 v27, v27\n" \
  "v_exp_f32 v28, v28\n" \
  "v_exp_f32 v29, v29\n" \
  "v_exp_f32 v30, v30\n" \
  "v_exp_f32 v31, v31\n" \
  "v_exp_f32 v32, v32\n" \
  "v_exp_f32 v33, v33\n" \
  "s_waitcnt vmcnt(26)\n" \
  "v_smfmac_f32_32x32x32_bf16 v[0:15], v[164:167], v[24:31], v56\n" \
  "s_add_u32 s40, s40, 4096\n" \
  "s_addc_u32 s41, s41, 0\n" \
  "global_load_dwordx4 v[120:123], v57, s[40:41] offset:0 sc1\n" \
  "global_load_dwordx4 v[124:127], v57, s[40:41] offset:2048 sc1\n" \
  "v_exp_f32 v34, v34\n" \
  "v_exp_f32 v35, v35\n" \
  "v_exp_f32 v36, v36\n" \
  "s_waitcnt vmcnt(25)\n" \
  "v_mfma_f32_32x32x16_bf16 v[16:31], v[88:91], v[48:51], 0\n" \
  "s_mov_b64 exec, s[44:45]\n" \
  "global_load_dwordx4 v[240:243], v58, s[40:41] offset:1024 sc1\n" \
  "global_load_dwordx4 v[244:247], v58, s[40:41] offset:1536 sc1\n" \
  "s_mov_b64 exec, -1\n" \
  "v_exp_f32 v37, v37\n" \
  "v_exp_f32 v38, v38\n" \
  "v_exp_f32 v39, v39\n" \
  "v_exp_f32 v40, v40\n" \
  "v_exp_f32 v41, v41\n" \
  "v_smfmac_f32_32x32x32_bf16 v[0:15], v[168:171], v[32:39], v56\n" \
  "s_mov_b64 exec, s[44:45]\n" \
  "global_load_dwordx4 v[248:251], v58, s[40:41] offset:3072 sc1\n" \
  "global_load_dwordx4 v[252:255], v58, s[40:41] offset:3584 sc1\n" \
  "s_mov_b64 exec, -1\n" \
  "v_exp_f32 v42, v42\n" \
  "v_exp_f32 v43, v43\n" \
  "v_exp_f32 v44, v44\n" \
  "v_exp_f32 v45, v45\n" \
  "v_exp_f32 v46, v46\n" \
  "v_exp_f32 v47, v47\n" \
  "v_exp_f32 v16, v16\n" \
  "v_exp_f32 v17, v17\n" \
  "v_smfmac_f32_32x32x32_bf16 v[0:15], v[172:175], v[40:47], v56\n" \
  "v_exp_f32 v18, v18\n" \
  "v_exp_f32 v19, v19\n" \
  "v_exp_f32 v20, v20\n" \
  "v_lshrrev_b32 v60, 4, v57\n" \
  "v_mul_lo_u32 v60, v60, %8\n" \
  "global_load_dword v61, v60, %7 sc1\n" \
  "ds_write_b32 v62, v56\n" \
  "s_waitcnt vmcnt(29)\n" \
  "v_mfma_f32_32x32x16_bf16 v[32:47], v[92:95], v[48:51], 0\n" \
  "v_exp_f32 v21, v21\n" \
  "v_exp_f32 v22, v22\n" \
  "v_exp_f32 v23, v23\n" \
  "v_exp_f32 v24, v24\n" \
  "v_exp_f32 v25, v25\n" \
  "s_waitcnt vmcnt(28)\n" \
  "v_smfmac_f32_32x32x32_bf16 v[0:15], v[176:179], v[16:23], v56\n" \
  "v_exp_f32 v26, v26\n" \
  "v_exp_f32 v27, v27\n" \
  "v_exp_f32 v28, v28\n" \
  "v_exp_f32 v29, v29\n" \
  "v_exp_f32 v30, v30\n" \
  "v_exp_f32 v31, v31\n" \
  "v_exp_f32 v32, v32\n" \
  "v_exp_f32 v33, v33\n" \
  "s_waitcnt vmcnt(27)\n" \
  "v_smfmac_f32_32x32x32_bf16 v[0:15], v[180:183], v[24:31], v56\n" \
  "v_exp_f32 v34, v34\n" \
  "v_exp_f32 v35, v35\n" \
  "v_exp_f32 v36, v36\n" \
  "v_mfma_f32_32x32x16_bf16 v[16:31], v[64:67], v[52:55], 0\n" \
  "v_exp_f32 v37, v37\n" \
  "v_exp_f32 v38, v38\n" \
  "v_exp_f32 v39, v39\n" \
  "v_exp_f32 v40, v40\n" \
  "v_exp_f32 v41, v41\n" \
  "s_waitcnt vmcnt(26)\n" \
  "v_smfmac_f32_32x32x32_bf16 v[0:15], v[184:187], v[32:39], v56\n" \
  "v_exp_f32 v42, v42\n" \
  "v_exp_f32 v43, v43\n" \
  "v_exp_f32 v44, v44\n" \
  "v_exp_f32 v45, v45\n" \
  "v_exp_f32 v46, v46\n" \
  "v_exp_f32 v47, v47\n" \
  "v_exp_f32 v16, v16\n" \
  "v_exp_f32 v17, v17\n" \
  "s_waitcnt vmcnt(25)\n" \
  "v_smfmac_f32_32x32x32_bf16 v[0:15], v[188:191], v[40:47], v56\n" \
  "v_exp_f32 v18, v18\n" \
  "v_exp_f32 v19, v19\n" \
  "v_exp_f32 v20, v20\n" \
  "v_mfma_f32_32x32x16_bf16 v[32:47], v[68:71], v[52:55], 0\n" \
  "v_exp_f32 v21, v21\n" \
  "v_exp_f32 v22, v22\n" \
  "v_exp_f32 v23, v23\n" \
  "v_exp_f32 v24, v24\n" \
  "v_exp_f32 v25, v25\n" \
  "v_exp_f32 v26, v26\n" \
  "v_exp_f32 v27, v27\n" \
  "v_exp_f32 v28, v28\n" \
  "ds_write_b128 v59, v[0:3]\n" \
  "ds_write_b128 v59, v[4:7] offset:1024\n" \
  "ds_write_b32 v59, v8 offset:2048\n" \
  "ds_write_b32 v62, v56\n" \
  "v_mov_b64 v[0:1], 0\n" \
  "v_mov_b64 v[2:3], 0\n" \
  "v_mov_b64 v[4:5], 0\n" \
  "v_mov_b64 v[6:7], 0\n" \
  "v_mov_b32 v8, 0\n" \
  "v_add_u32 v59, -12288, v59\n" \
  "v_add_u32 v62, 32, v62\n" \
  "v_exp_f32 v29, v29\n" \
  "v_exp_f32 v30, v30\n" \
  "v_smfmac_f32_32x32x32_bf16 v[0:15], v[128:131], v[16:23], v56\n" \
  "v_exp_f32 v31, v31\n" \
  "v_exp_f32 v32, v32\n" \
  "v_exp_f32 v33, v33\n" \
  "v_smfmac_f32_32x32x32_bf16 v[0:15], v[132:135], v[24:31], v56\n" \
  "v_exp_f32 v34, v34\n" \
  "v_exp_f32 v35, v35\n" \
  "v_exp_f32 v36, v36\n" \
  "v_mfma_f32_32x32x16_bf16 v[16:31], v[72:75], v[52:55], 0\n" \
  "v_exp_f32 v37, v37\n" \
  "v_exp_f32 v38, v38\n" \
  "v_exp_f32 v39, v39\n" \
  "v_exp_f32 v40, v40\n" \
  "v_exp_f32 v41, v41\n" \
  "v_smfmac_f32_32x32x32_bf16 v[0:15], v[136:139], v[32:39], v56\n" \
  "v_exp_f32 v42, v42\n" \
  "v_exp_f32 v43, v43\n" \
  "v_exp_f32 v44, v44\n" \
  "v_exp_f32 v45, v45\n" \
  "v_exp_f32 v46, v46\n" \
  "v_exp_f32 v47, v47\n" \
  "v_exp_f32 v16, v16\n" \
  "v_exp_f32 v17, v17\n" \
  "v_smfmac_f32_32x32x32_bf16 v[0:15], v[140:143], v[40:47], v56\n" \
  "v_exp_f32 v18, v18\n" \
  "v_exp_f32 v19, v19\n" \
  "v_exp_f32 v20, v20\n" \
  "v_mfma_f32_32x32x16_bf16 v[32:47], v[76:79], v[52:55], 0\n" \
  "v_exp_f32 v21, v21\n" \
  "v_exp_f32 v22, v22\n" \
  "v_exp_f32 v23, v23\n" \
  "v_exp_f32 v24, v24\n" \
  "v_exp_f32 v25, v25\n" \
  "v_smfmac_f32_32x32x32_bf16 v[0:15], v[144:147], v[16:23], v56\n" \
  "v_exp_f32 v26, v26\n" \
  "v_exp_f32 v27, v27\n" \
  "v_exp_f32 v28, v28\n" \
  "v_exp_f32 v29, v29\n" \
  "v_exp_f32 v30, v30\n" \
  "v_exp_f32 v31, v31\n" \
  "v_exp_f32 v32, v32\n" \
  "v_exp_f32 v33, v33\n" \
  "v_smfmac_f32_32x32x32_bf16 v[0:15], v[148:151], v[24:31], v56\n" \
  "v_exp_f32 v34, v34\n" \
  "v_exp_f32 v35, v35\n" \
  "v_exp_f32 v36, v36\n" \
  "v_mfma_f32_32x32x16_bf16 v[16:31], v[80:83], v[52:55], 0\n" \
  "v_exp_f32 v37, v37\n" \
  "v_exp_f32 v38, v38\n" \
  "v_exp_f32 v39, v39\n" \
  "v_exp_f32 v40, v40\n" \
  "v_exp_f32 v41, v41\n" \
  "v_smfmac_f32_32x32x32_bf16 v[0:15], v[152:155], v[32:39], v56\n" \
  "v_exp_f32 v42, v42\n" \
  "v_exp_f32 v43, v43\n" \
  "v_exp_f32 v44, v44\n" \
  "v_exp_f32 v45, v45\n" \
  "v_exp_f32 v46, v46\n" \
  "v_exp_f32 v47, v47\n" \
  "v_exp_f32 v16, v16\n" \
  "v_exp_f32 v17, v17\n" \
  "v_smfmac_f32_32x32x32_bf16 v[0:15], v[156:159], v[40:47], v56\n" \
  "v_exp_f32 v18, v18\n" \
  "v_exp_f32 v19, v19\n" \
  "v_exp_f32 v20, v20\n" \
  "v_mfma_f32_32x32x16_bf16 v[32:47], v[84:87], v[52:55], 0\n" \
  "v_exp_f32 v21, v21\n" \
  "v_exp_f32 v22, v22\n" \
  "v_exp_f32 v23, v23\n" \
  "v_exp_f32 v24, v24\n" \
  "v_exp_f32 v25, v25\n" \
  "v_smfmac_f32_32x32x32_bf16 v[0:15], v[160:163], v[16:23], v56\n" \
  "v_exp_f32 v26, v26\n" \
  "v_exp_f32 v27, v27\n" \
  "v_exp_f32 v28, v28\n" \
  "v_exp_f32 v29, v29\n" \
  "v_exp_f32 v30, v30\n" \
  "v_exp_f32 v31, v31\n" \
  "v_exp_f32 v32, v32\n" \
  "v_exp_f32 v33, v33\n" \
  "v_smfmac_f32_32x32x32_bf16 v[0:15], v[164:167], v[24:31], v56\n" \
  "v_exp_f32 v34, v34\n" \
  "v_exp_f32 v35, v35\n" \
  "v_exp_f32 v36, v36\n" \
  "v_mfma_f32_32x32x16_bf16 v[16:31], v[88:91], v[52:55], 0\n" \
  "v_exp_f32 v37, v37\n" \
  "v_exp_f32 v38, v38\n" \
  "v_exp_f32 v39, v39\n" \
  "v_exp_f32 v40, v40\n" \
  "v_exp_f32 v41, v41\n" \
  "v_smfmac_f32_32x32x32_bf16 v[0:15], v[168:171], v[32:39], v56\n" \
  "v_exp_f32 v42, v42\n" \
  "v_exp_f32 v43, v43\n" \
  "v_exp_f32 v44, v44\n" \
  "v_exp_f32 v45, v45\n" \
  "v_exp_f32 v46, v46\n" \
  "v_exp_f32 v47, v47\n" \
  "v_exp_f32 v16, v16\n" \
  "v_exp_f32 v17, v17\n" \
  "v_smfmac_f32_32x32x32_bf16 v[0:15], v[172:175], v[40:47], v56\n" \
  "v_exp_f32 v18, v18\n" \
  "v_exp_f32 v19, v19\n" \
  "v_exp_f32 v20, v20\n" \
  "v_mfma_f32_32x32x16_bf16 v[32:47], v[92:95], v[52:55], 0\n" \
  "v_exp_f32 v21, v21\n" \
  "v_exp_f32 v22, v22\n" \
  "v_exp_f32 v23, v23\n" \
  "v_exp_f32 v24, v24\n" \
  "v_exp_f32 v25, v25\n" \
  "v_smfmac_f32_32x32x32_bf16 v[0:15], v[176:179], v[16:23], v56\n" \
  "v_exp_f32 v26, v26\n" \
  "v_exp_f32 v27, v27\n" \
  "v_exp_f32 v28, v28\n" \
  "v_exp_f32 v29, v29\n" \
  "v_exp_f32 v30, v30\n" \
  "v_exp_f32 v31, v31\n" \
  "v_exp_f32 v32, v32\n" \
  "v_exp_f32 v33, v33\n" \
  "v_smfmac_f32_32x32x32_bf16 v[0:15], v[180:183], v[24:31], v56\n" \
  "v_exp_f32 v34, v34\n" \
  "v_exp_f32 v35, v35\n" \
  "v_exp_f32 v36, v36\n" \
  "s_waitcnt vmcnt(24)\n" \
  "v_mfma_f32_32x32x16_bf16 v[16:31], v[96:99], v[48:51], 0\n" \
  "v_exp_f32 v37, v37\n" \
  "v_exp_f32 v38, v38\n" \
  "v_exp_f32 v39, v39\n" \
  "v_exp_f32 v40, v40\n" \
  "v_exp_f32 v41, v41\n" \
  "v_smfmac_f32_32x32x32_bf16 v[0:15], v[184:187], v[32:39], v56\n" \
  "v_exp_f32 v42, v42\n" \
  "v_exp_f32 v43, v43\n" \
  "v_exp_f32 v44, v44\n" \
  "v_exp_f32 v45, v45\n" \
  "v_exp_f32 v46, v46\n" \
  "v_exp_f32 v47, v47\n" \
  "v_exp_f32 v16, v16\n" \
  "v_exp_f32 v17, v17\n" \
  "v_smfmac_f32_32x32x32_bf16 v[0:15], v[188:191], v[40:47], v56\n" \
  "v_exp_f32 v18, v18\n" \
  "v_exp_f32 v19, v19\n" \
  "v_exp_f32 v20, v20\n" \
  "s_waitcnt vmcnt(23)\n" \
  "v_mfma_f32_32x32x16_bf16 v[32:47], v[100:103], v[48:51], 0\n" \
  "v_and_b32 v58, 0xffffffe0, v62\n" \
  "v_add_u32 v58, 48, v58\n" \
  "v_exp_f32 v21, v21\n" \
  "v_exp_f32 v22, v22\n" \
  "v_exp_f32 v23, v23\n" \
  "v_exp_f32 v24, v24\n" \
  "v_exp_f32 v25, v25\n" \
  "v_exp_f32 v26, v26\n" \
  "v_exp_f32 v27, v27\n" \
  "v_exp_f32 v28, v28\n" \
  "ds_write_b128 v59, v[0:3]\n" \
  "ds_write_b128 v59, v[4:7] offset:1024\n" \
  "ds_write_b32 v59, v8 offset:2048\n" \
  "ds_write_b32 v62, v56\n" \
  "v_mov_b64 v[0:1], 0\n" \
  "v_mov_b64 v[2:3], 0\n" \
  "v_mov_b64 v[4:5], 0\n" \
  "v_mov_b64 v[6:7], 0\n" \
  "v_mov_b32 v8, 0\n" \
  "v_add_u32 v59, -12288, v59\n" \
  "v_add_u32 v62, 32, v62\n" \
  "v_exp_f32 v29, v29\n" \
  "v_exp_f32 v30, v30\n" \
  "s_waitcnt vmcnt(22)\n" \
  "v_smfmac_f32_32x32x32_bf16 v[0:15], v[192:195], v[16:23], v56\n" \
  "v_exp_f32 v31, v31\n" \
  "v_exp_f32 v32, v32\n" \
  "v_exp_f32 v33, v33\n" \
  "s_waitcnt vmcnt(21)\n" \
  "v_smfmac_f32_32x32x32_bf16 v[0:15], v[196:199], v[24:31], v56\n" \
  "v_exp_f32 v34, v34\n" \
  "v_exp_f32 v35, v35\n" \
  "v_exp_f32 v36, v36\n" \
  "s_waitcnt vmcnt(18)\n" \
  "v_mfma_f32_32x32x16_bf16 v[16:31], v[104:107], v[48:51], 0\n" \
  "v_exp_f32 v37, v37\n" \
  "v_exp_f32 v38, v38\n" \
  "v_exp_f32 v39, v39\n" \
  "v_exp_f32 v40, v40\n" \
  "v_exp_f32 v41, v41\n" \
  "v_smfmac_f32_32x32x32_bf16 v[0:15], v[200:203], v[32:39], v56\n" \
  "v_exp_f32 v42, v42\n" \
  "v_exp_f32 v43, v43\n" \
  "v_exp_f32 v44, v44\n" \
  "v_exp_f32 v45, v45\n" \
  "v_exp_f32 v46, v46\n" \
  "v_exp_f32 v47, v47\n" \
  "v_exp_f32 v16, v16\n" \
  "v_exp_f32 v17, v17\n" \
  "v_smfmac_f32_32x32x32_bf16 v[0:15], v[204:207], v[40:47], v56\n" \
  "v_exp_f32 v18, v18\n" \
  "v_exp_f32 v19, v19\n" \
  "v_exp_f32 v20, v20\n" \
  "s_waitcnt vmcnt(17)\n" \
  "v_mfma_f32_32x32x16_bf16 v[32:47], v[108:111], v[48:51], 0\n" \
  "v_exp_f32 v21, v21\n" \
  "v_exp_f32 v22, v22\n" \
  "v_exp_f32 v23, v23\n" \
  "v_exp_f32 v24, v24\n" \
  "v_exp_f32 v25, v25\n" \
  "s_waitcnt vmcnt(16)\n" \
  "v_smfmac_f32_32x32x32_bf16 v[0:15], v[208:211], v[16:23], v56\n" \
  "v_exp_f32 v26, v26\n" \
  "v_exp_f32 v27, v27\n" \
  "v_exp_f32 v28, v28\n" \
  "v_exp_f32 v29, v29\n" \
  "v_exp_f32 v30, v30\n" \
  "v_exp_f32 v31, v31\n" \
  "v_exp_f32 v32, v32\n" \
  "v_exp_f32 v33, v33\n" \
  "s_waitcnt vmcnt(15)\n" \
  "v_smfmac_f32_32x32x32_bf16 v[0:15], v[212:215], v[24:31], v56\n" \
  "v_exp_f32 v34, v34\n" \
  "v_exp_f32 v35, v35\n" \
  "v_exp_f32 v36, v36\n" \
  "s_waitcnt vmcnt(12)\n" \
  "v_mfma_f32_32x32x16_bf16 v[16:31], v[112:115], v[48:51], 0\n" \
  "v_exp_f32 v37, v37\n" \
  "v_exp_f32 v38, v38\n" \
  "v_exp_f32 v39, v39\n" \
  "v_exp_f32 v40, v40\n" \
  "v_exp_f32 v41, v41\n" \
  "v_smfmac_f32_32x32x32_bf16 v[0:15], v[216:219], v[32:39], v56\n" \
  "v_exp_f32 v42, v42\n" \
  "v_exp_f32 v43, v43\n" \
  "v_exp_f32 v44, v44\n" \
  "v_exp_f32 v45, v45\n" \
  "v_exp_f32 v46, v46\n" \
  "v_exp_f32 v47, v47\n" \
  "v_exp_f32 v16, v16\n" \
  "v_exp_f32 v17, v17\n" \
  "v_smfmac_f32_32x32x32_bf16 v[0:15], v[220:223], v[40:47], v56\n" \
  "v_exp_f32 v18, v18\n" \
  "v_exp_f32 v19, v19\n" \
  "v_exp_f32 v20, v20\n" \
  "s_waitcnt vmcnt(11)\n" \
  "v_mfma_f32_32x32x16_bf16 v[32:47], v[116:119], v[48:51], 0\n" \
  "v_exp_f32 v21, v21\n" \
  "v_exp_f32 v22, v22\n" \
  "v_exp_f32 v23, v23\n" \
  "v_exp_f32 v24, v24\n" \
  "v_exp_f32 v25, v25\n" \
  "s_waitcnt vmcnt(10)\n" \
  "v_smfmac_f32_32x32x32_bf16 v[0:15], v[224:227], v[16:23], v56\n" \
  "v_exp_f32 v26, v26\n" \
  "v_exp_f32 v27, v27\n" \
  "v_exp_f32 v28, v28\n" \
  "v_exp_f32 v29, v29\n" \
  "v_exp_f32 v30, v30\n" \
  "v_exp_f32 v31, v31\n" \
  "v_exp_f32 v32, v32\n" \
  "v_exp_f32 v33, v33\n" \
  "s_waitcnt vmcnt(9)\n" \
  "v_smfmac_f32_32x32x32_bf16 v[0:15], v[228:231], v[24:31], v56\n" \
  "v_exp_f32 v34, v34\n" \
  "v_exp_f32 v35, v35\n" \
  "v_exp_f32 v36, v36\n" \
  "s_waitcnt vmcnt(6)\n" \
  "v_mfma_f32_32x32x16_bf16 v[16:31], v[120:123], v[48:51], 0\n" \
  "v_exp_f32 v37, v37\n" \
  "v_exp_f32 v38, v38\n" \
  "v_exp_f32 v39, v39\n" \
  "v_exp_f32 v40, v40\n" \
  "v_exp_f32 v41, v41\n" \
  "v_smfmac_f32_32x32x32_bf16 v[0:15], v[232:235], v[32:39], v56\n" \
  "v_exp_f32 v42, v42\n" \
  "v_exp_f32 v43, v43\n" \
  "v_exp_f32 v44, v44\n" \
  "v_exp_f32 v45, v45\n" \
  "v_exp_f32 v46, v46\n" \
  "v_exp_f32 v47, v47\n" \
  "v_exp_f32 v16, v16\n" \
  "v_exp_f32 v17, v17\n" \
  "v_smfmac_f32_32x32x32_bf16 v[0:15], v[236:239], v[40:47], v56\n" \
  "v_exp_f32 v18, v18\n" \
  "v_exp_f32 v19, v19\n" \
  "v_exp_f32 v20, v20\n" \
  "s_waitcnt vmcnt(5)\n" \
  "v_mfma_f32_32x32x16_bf16 v[32:47], v[124:127], v[48:51], 0\n" \
  "v_exp_f32 v21, v21\n" \
  "v_exp_f32 v22, v22\n" \
  "v_exp_f32 v23, v23\n" \
  "v_exp_f32 v24, v24\n" \
  "v_exp_f32 v25, v25\n" \
  "s_waitcnt vmcnt(4)\n" \
  "v_smfmac_f32_32x32x32_bf16 v[0:15], v[240:243], v[16:23], v56\n" \
  "v_exp_f32 v26, v26\n" \
  "v_exp_f32 v27, v27\n" \
  "v_exp_f32 v28, v28\n" \
  "v_exp_f32 v29, v29\n" \
  "v_exp_f32 v30, v30\n" \
  "v_exp_f32 v31, v31\n" \
  "v_exp_f32 v32, v32\n" \
  "v_exp_f32 v33, v33\n" \
  "s_waitcnt vmcnt(3)\n" \
  "v_smfmac_f32_32x32x32_bf16 v[0:15], v[244:247], v[24:31], v56\n" \
  "v_exp_f32 v34, v34\n" \
  "v_exp_f32 v35, v35\n" \
  "v_exp_f32 v36, v36\n" \
  "v_mfma_f32_32x32x16_bf16 v[16:31], v[96:99], v[52:55], 0\n" \
  "v_exp_f32 v37, v37\n" \
  "v_exp_f32 v38, v38\n" \
  "v_exp_f32 v39, v39\n" \
  "v_exp_f32 v40, v40\n" \
  "v_exp_f32 v41, v41\n" \
  "s_waitcnt vmcnt(2)\n" \
  "v_smfmac_f32_32x32x32_bf16 v[0:15], v[248:251], v[32:39], v56\n" \
  "v_exp_f32 v42, v42\n" \
  "v_exp_f32 v43, v43\n" \
  "v_exp_f32 v44, v44\n" \
  "v_exp_f32 v45, v45\n" \
  "v_exp_f32 v46, v46\n" \
  "v_exp_f32 v47, v47\n" \
  "v_exp_f32 v16, v16\n" \
  "v_exp_f32 v17, v17\n" \
  "s_waitcnt vmcnt(1)\n" \
  "v_smfmac_f32_32x32x32_bf16 v[0:15], v[252:255], v[40:47], v56\n" \
  "s_waitcnt vmcnt(0)\n" \
  "ds_read_b64 v[60:61], v58\n" \
  "v_exp_f32 v18, v18\n" \
  "v_exp_f32 v19, v19\n" \
  "v_exp_f32 v20, v20\n" \
  "v_mfma_f32_32x32x16_bf16 v[32:47], v[100:103], v[52:55], 0\n" \
  "v_exp_f32 v21, v21\n" \
  "v_exp_f32 v22, v22\n" \
  "v_exp_f32 v23, v23\n" \
  "v_exp_f32 v24, v24\n" \
  "v_exp_f32 v25, v25\n" \
  "v_exp_f32 v26, v26\n" \
  "v_exp_f32 v27, v27\n" \
  "v_exp_f32 v28, v28\n" \
  "Lcw_chk0_%=:\n" \
  "s_waitcnt lgkmcnt(0)\n" \
  "v_and_b32 v60, v60, v61\n" \
  "v_cmp_eq_u32 vcc, 0, v60\n" \
  "s_cbranch_vccnz Lcw_spin0_%=\n" \
  "ds_write_b128 v59, v[0:3]\n" \
  "ds_write_b128 v59, v[4:7] offset:1024\n" \
  "ds_write_b32 v59, v8 offset:2048\n" \
  "ds_write_b32 v62, v56\n" \
  "v_mov_b64 v[0:1], 0\n" \
  "v_mov_b64 v[2:3], 0\n" \
  "v_mov_b64 v[4:5], 0\n" \
  "v_mov_b64 v[6:7], 0\n" \
  "v_mov_b32 v8, 0\n" \
  "v_add_u32 v59, -12288, v59\n" \
  "v_add_u32 v62, 32, v62\n" \
  "v_add_u32 v58, 32, v58\n" \
  "ds_read_b128 v[48:51], v63 offset:2048\n" \
  "v_exp_f32 v29, v29\n" \
  "v_exp_f32 v30, v30\n" \
  "v_smfmac_f32_32x32x32_bf16 v[0:15], v[192:195], v[16:23], v56\n" \
  "v_exp_f32 v31, v31\n" \
  "v_exp_f32 v32, v32\n" \
  "v_exp_f32 v33, v33\n" \
  "v_smfmac_f32_32x32x32_bf16 v[0:15], v[196:199], v[24:31], v56\n" \
  "v_exp_f32 v34, v34\n" \
  "v_exp_f32 v35, v35\n" \
  "v_exp_f32 v36, v36\n" \
  "v_mfma_f32_32x32x16_bf16 v[16:31], v[104:107], v[52:55], 0\n" \
  "v_exp_f32 v37, v37\n" \
  "v_exp_f32 v38, v38\n" \
  "v_exp_f32 v39, v39\n" \
  "v_exp_f32 v40, v40\n" \
  "v_exp_f32 v41, v41\n" \
  "v_smfmac_f32_32x32x32_bf16 v[0:15], v[200:203], v[32:39], v56\n" \
  "v_exp_f32 v42, v42\n" \
  "v_exp_f32 v43, v43\n" \
  "v_exp_f32 v44, v44\n" \
  "v_exp_f32 v45, v45\n" \
  "v_exp_f32 v46, v46\n" \
  "v_exp_f32 v47, v47\n" \
  "v_exp_f32 v16, v16\n" \
  "v_exp_f32 v17, v17\n" \
  "v_smfmac_f32_32x32x32_bf16 v[0:15], v[204:207], v[40:47], v56\n" \
  "v_exp_f32 v18, v18\n" \
  "v_exp_f32 v19, v19\n" \
  "v_exp_f32 v20, v20\n" \
  "v_mfma_f32_32x32x16_bf16 v[32:47], v[108:111], v[52:55], 0\n" \
  "v_exp_f32 v21, v21\n" \
  "v_exp_f32 v22, v22\n" \
  "v_exp_f32 v23, v23\n" \
  "v_exp_f32 v24, v24\n" \
  "v_exp_f32 v25, v25\n" \
  "v_smfmac_f32_32x32x32_bf16 v[0:15], v[208:211], v[16:23], v56\n" \
  "v_exp_f32 v26, v26\n" \
  "v_exp_f32 v27, v27\n" \
  "v_exp_f32 v28, v28\n" \
  "v_exp_f32 v29, v29\n" \
  "v_exp_f32 v30, v30\n" \
  "v_exp_f32 v31, v31\n" \
  "v_exp_f32 v32, v32\n" \
  "v_exp_f32 v33, v33\n" \
  "v_smfmac_f32_32x32x32_bf16 v[0:15], v[212:215], v[24:31], v56\n" \
  "v_exp_f32 v34, v34\n" \
  "v_exp_f32 v35, v35\n" \
  "v_exp_f32 v36, v36\n" \
  "v_mfma_f32_32x32x16_bf16 v[16:31], v[112:115], v[52:55], 0\n" \
  "v_exp_f32 v37, v37\n" \
  "v_exp_f32 v38, v38\n" \
  "v_exp_f32 v39, v39\n" \
  "v_exp_f32 v40, v40\n" \
  "v_exp_f32 v41, v41\n" \
  "v_smfmac_f32_32x32x32_bf16 v[0:15], v[216:219], v[32:39], v56\n" \
  "v_exp_f32 v42, v42\n" \
  "v_exp_f32 v43, v43\n" \
  "v_exp_f32 v44, v44\n" \
  "v_exp_f32 v45, v45\n" \
  "v_exp_f32 v46, v46\n" \
  "v_exp_f32 v47, v47\n" \
  "v_exp_f32 v16, v16\n" \
  "v_exp_f32 v17, v17\n" \
  "v_smfmac_f32_32x32x32_bf16 v[0:15], v[220:223], v[40:47], v56\n" \
  "v_exp_f32 v18, v18\n" \
  "v_exp_f32 v19, v19\n" \
  "v_exp_f32 v20, v20\n" \
  "v_mfma_f32_32x32x16_bf16 v[32:47], v[116:119], v[52:55], 0\n" \
  "v_exp_f32 v21, v21\n" \
  "v_exp_f32 v22, v22\n" \
  "v_exp_f32 v23, v23\n" \
  "v_exp_f32 v24, v24\n" \
  "v_exp_f32 v25, v25\n" \
  "v_smfmac_f32_32x32x32_bf16 v[0:15], v[224:227], v[16:23], v56\n" \
  "v_exp_f32 v26, v26\n" \
  "v_exp_f32 v27, v27\n" \
  "v_exp_f32 v28, v28\n" \
  "v_exp_f32 v29, v29\n" \
  "v_exp_f32 v30, v30\n" \
  "v_exp_f32 v31, v31\n" \
  "v_exp_f32 v32, v32\n" \
  "v_exp_f32 v33, v33\n" \
  "v_smfmac_f32_32x32x32_bf16 v[0:15], v[228:231], v[24:31], v56\n" \
  "v_exp_f32 v34, v34\n" \
  "v_exp_f32 v35, v35\n" \
  "v_exp_f32 v36, v36\n" \
  "v_mfma_f32_32x32x16_bf16 v[16:31], v[120:123], v[52:55], 0\n" \
  "v_exp_f32 v37, v37\n" \
  "v_exp_f32 v38, v38\n" \
  "v_exp_f32 v39, v39\n" \
  "v_exp_f32 v40, v40\n" \
  "v_exp_f32 v41, v41\n" \
  "v_smfmac_f32_32x32x32_bf16 v[0:15], v[232:235], v[32:39], v56\n" \
  "v_exp_f32 v42, v42\n" \
  "v_exp_f32 v43, v43\n" \
  "v_exp_f32 v44, v44\n" \
  "v_exp_f32 v45, v45\n" \
  "v_exp_f32 v46, v46\n" \
  "v_exp_f32 v47, v47\n" \
  "v_exp_f32 v16, v16\n" \
  "v_exp_f32 v17, v17\n" \
  "v_smfmac_f32_32x32x32_bf16 v[0:15], v[236:239], v[40:47], v56\n" \
  "v_exp_f32 v18, v18\n" \
  "v_exp_f32 v19, v19\n" \
  "v_exp_f32 v20, v20\n" \
  "v_mfma_f32_32x32x16_bf16 v[32:47], v[124:127], v[52:55], 0\n" \
  "v_exp_f32 v21, v21\n" \
  "v_exp_f32 v22, v22\n" \
  "v_exp_f32 v23, v23\n" \
  "v_exp_f32 v24, v24\n" \
  "v_exp_f32 v25, v25\n" \
  "v_smfmac_f32_32x32x32_bf16 v[0:15], v[240:243], v[16:23], v56\n" \
  "v_exp_f32 v26, v26\n" \
  "v_exp_f32 v27, v27\n" \
  "v_exp_f32 v28, v28\n" \
  "v_exp_f32 v29, v29\n" \
  "v_exp_f32 v30, v30\n" \
  "v_exp_f32 v31, v31\n" \
  "v_exp_f32 v32, v32\n" \
  "v_exp_f32 v33, v33\n" \
  "v_smfmac_f32_32x32x32_bf16 v[0:15], v[244:247], v[24:31], v56\n" \
  "v_exp_f32 v34, v34\n" \
  "v_exp_f32 v35, v35\n" \
  "v_exp_f32 v36, v36\n" \
  "s_waitcnt lgkmcnt(0)\n" \
  "v_mfma_f32_32x32x16_bf16 v[16:31], v[64:67], v[48:51], 0\n" \
  "v_exp_f32 v37, v37\n" \
  "v_exp_f32 v38, v38\n" \
  "v_exp_f32 v39, v39\n" \
  "v_exp_f32 v40, v40\n" \
  "v_exp_f32 v41, v41\n" \
  "v_smfmac_f32_32x32x32_bf16 v[0:15], v[248:251], v[32:39], v56\n" \
  "v_exp_f32 v42, v42\n" \
  "v_exp_f32 v43, v43\n" \
  "v_exp_f32 v44, v44\n" \
  "v_exp_f32 v45, v45\n" \
  "v_exp_f32 v46, v46\n" \
  "v_exp_f32 v47, v47\n" \
  "s_mov_b32 s48, 6\n" \
  "s_mov_b32 s49, 36864\n" \
  "s_mov_b32 s47, 36864\n" \
  "s_mov_b32 s46, 0\n" \
  "Lcw_loop%=:\n" \
  "v_exp_f32 v16, v16\n" \
  "v_exp_f32 v17, v17\n" \
  "v_smfmac_f32_32x32x32_bf16 v[0:15], v[252:255], v[40:47], v56\n" \
  "ds_read_b64 v[60:61], v58\n" \
  "v_exp_f32 v18, v18\n" \
  "v_exp_f32 v19, v19\n" \
  "v_exp_f32 v20, v20\n" \
  "v_mfma_f32_32x32x16_bf16 v[32:47], v[68:71], v[48:51], 0\n" \
  "v_exp_f32 v21, v21\n" \
  "v_exp_f32 v22, v22\n" \
  "v_exp_f32 v23, v23\n" \
  "v_exp_f32 v24, v24\n" \
  "v_exp_f32 v25, v25\n" \
  "v_exp_f32 v26, v26\n" \
  "v_exp_f32 v27, v27\n" \
  "v_exp_f32 v28, v28\n" \
  "Lcw_chk1_%=:\n" \
  "s_waitcnt lgkmcnt(0)\n" \
  "v_and_b32 v60, v60, v61\n" \
  "v_cmp_eq_u32 vcc, 0, v60\n" \
  "s_cbranch_vccnz Lcw_spin1_%=\n" \
  "ds_write_b128 v59, v[0:3]\n" \
  "ds_write_b128 v59, v[4:7] offset:1024\n" \
  "ds_write_b32 v59, v8 offset:2048\n" \
  "ds_write_b32 v62, v56\n" \
  "v_mov_b64 v[0:1], 0\n" \
  "v_mov_b64 v[2:3], 0\n" \
  "v_mov_b64 v[4:5], 0\n" \
  "v_mov_b64 v[6:7], 0\n" \
  "v_mov_b32 v8, 0\n" \
  "v_add_u32 v59, s49, v59\n" \
  "v_add_u32 v62, 32, v62\n" \
  "v_add_u32 v58, 32, v58\n" \
  "ds_read_b128 v[52:55], v63 offset:3072\n" \
  "v_add_u32 v63, 1024, v63\n" \
  "s_add_u32 s46, s46, 1\n" \
  "s_and_b32 s46, s46, 3\n" \
  "s_cselect_b32 s49, -12288, s47\n" \
  "v_exp_f32 v29, v29\n" \
  "v_exp_f32 v30, v30\n" \
  "v_smfmac_f32_32x32x32_bf16 v[0:15], v[128:131], v[16:23], v56\n" \
  "v_exp_f32 v31, v31\n" \
  "v_exp_f32 v32, v32\n" \
  "v_exp_f32 v33, v33\n" \
  "v_smfmac_f32_32x32x32_bf16 v[0:15], v[132:135], v[24:31], v56\n" \
  "v_exp_f32 v34, v34\n" \
  "v_exp_f32 v35, v35\n" \
  "v_exp_f32 v36, v36\n" \
  "v_mfma_f32_32x32x16_bf16 v[16:31], v[72:75], v[48:51], 0\n" \
  "v_exp_f32 v37, v37\n" \
  "v_exp_f32 v38, v38\n" \
  "v_exp_f32 v39, v39\n" \
  "v_exp_f32 v40, v40\n" \
  "v_exp_f32 v41, v41\n" \
  "v_smfmac_f32_32x32x32_bf16 v[0:15], v[136:139], v[32:39], v56\n" \
  "v_exp_f32 v42, v42\n" \
  "v_exp_f32 v43, v43\n" \
  "v_exp_f32 v44, v44\n" \
  "v_exp_f32 v45, v45\n" \
  "v_exp_f32 v46, v46\n" \
  "v_exp_f32 v47, v47\n" \
  "v_exp_f32 v16, v16\n" \
  "v_exp_f32 v17, v17\n" \
  "v_smfmac_f32_32x32x32_bf16 v[0:15], v[140:143], v[40:47], v56\n" \
  "v_exp_f32 v18, v18\n" \
  "v_exp_f32 v19, v19\n" \
  "v_exp_f32 v20, v20\n" \
  "v_mfma_f32_32x32x16_bf16 v[32:47], v[76:79], v[48:51], 0\n" \
  "v_exp_f32 v21, v21\n" \
  "v_exp_f32 v22, v22\n" \
  "v_exp_f32 v23, v23\n" \
  "v_exp_f32 v24, v24\n" \
  "v_exp_f32 v25, v25\n" \
  "v_smfmac_f32_32x32x32_bf16 v[0:15], v[144:147], v[16:23], v56\n" \
  "v_exp_f32 v26, v26\n" \
  "v_exp_f32 v27, v27\n" \
  "v_exp_f32 v28, v28\n" \
  "v_exp_f32 v29, v29\n" \
  "v_exp_f32 v30, v30\n" \
  "v_exp_f32 v31, v31\n" \
  "v_exp_f32 v32, v32\n" \
  "v_exp_f32 v33, v33\n" \
  "v_smfmac_f32_32x32x32_bf16 v[0:15], v[148:151], v[24:31], v56\n" \
  "v_exp_f32 v34, v34\n" \
  "v_exp_f32 v35, v35\n" \
  "v_exp_f32 v36, v36\n" \
  "v_mfma_f32_32x32x16_bf16 v[16:31], v[80:83], v[48:51], 0\n" \
  "v_exp_f32 v37, v37\n" \
  "v_exp_f32 v38, v38\n" \
  "v_exp_f32 v39, v39\n" \
  "v_exp_f32 v40, v40\n" \
  "v_exp_f32 v41, v41\n" \
  "v_smfmac_f32_32x32x32_bf16 v[0:15], v[152:155], v[32:39], v56\n" \
  "v_exp_f32 v42, v42\n" \
  "v_exp_f32 v43, v43\n" \
  "v_exp_f32 v44, v44\n" \
  "v_exp_f32 v45, v45\n" \
  "v_exp_f32 v46, v46\n" \
  "v_exp_f32 v47, v47\n" \
  "v_exp_f32 v16, v16\n" \
  "v_exp_f32 v17, v17\n" \
  "v_smfmac_f32_32x32x32_bf16 v[0:15], v[156:159], v[40:47], v56\n" \
  "v_exp_f32 v18, v18\n" \
  "v_exp_f32 v19, v19\n" \
  "v_exp_f32 v20, v20\n" \
  "v_mfma_f32_32x32x16_bf16 v[32:47], v[84:87], v[48:51], 0\n" \
  "v_exp_f32 v21, v21\n" \
  "v_exp_f32 v22, v22\n" \
  "v_exp_f32 v23, v23\n" \
  "v_exp_f32 v24, v24\n" \
  "v_exp_f32 v25, v25\n" \
  "v_smfmac_f32_32x32x32_bf16 v[0:15], v[160:163], v[16:23], v56\n" \
  "v_exp_f32 v26, v26\n" \
  "v_exp_f32 v27, v27\n" \
  "v_exp_f32 v28, v28\n" \
  "v_exp_f32 v29, v29\n" \
  "v_exp_f32 v30, v30\n" \
  "v_exp_f32 v31, v31\n" \
  "v_exp_f32 v32, v32\n" \
  "v_exp_f32 v33, v33\n" \
  "v_smfmac_f32_32x32x32_bf16 v[0:15], v[164:167], v[24:31], v56\n" \
  "v_exp_f32 v34, v34\n" \
  "v_exp_f32 v35, v35\n" \
  "v_exp_f32 v36, v36\n" \
  "v_mfma_f32_32x32x16_bf16 v[16:31], v[88:91], v[48:51], 0\n" \
  "v_exp_f32 v37, v37\n" \
  "v_exp_f32 v38, v38\n" \
  "v_exp_f32 v39, v39\n" \
  "v_exp_f32 v40, v40\n" \
  "v_exp_f32 v41, v41\n" \
  "v_smfmac_f32_32x32x32_bf16 v[0:15], v[168:171], v[32:39], v56\n" \
  "v_exp_f32 v42, v42\n" \
  "v_exp_f32 v43, v43\n" \
  "v_exp_f32 v44, v44\n" \
  "v_exp_f32 v45, v45\n" \
  "v_exp_f32 v46, v46\n" \
  "v_exp_f32 v47, v47\n" \
  "v_exp_f32 v16, v16\n" \
  "v_exp_f32 v17, v17\n" \
  "v_smfmac_f32_32x32x32_bf16 v[0:15], v[172:175], v[40:47], v56\n" \
  "v_exp_f32 v18, v18\n" \
  "v_exp_f32 v19, v19\n" \
  "v_exp_f32 v20, v20\n" \
  "v_mfma_f32_32x32x16_bf16 v[32:47], v[92:95], v[48:51], 0\n" \
  "v_exp_f32 v21, v21\n" \
  "v_exp_f32 v22, v22\n" \
  "v_exp_f32 v23, v23\n" \
  "v_exp_f32 v24, v24\n" \
  "v_exp_f32 v25, v25\n" \
  "v_smfmac_f32_32x32x32_bf16 v[0:15], v[176:179], v[16:23], v56\n" \
  "v_exp_f32 v26, v26\n" \
  "v_exp_f32 v27, v27\n" \
  "v_exp_f32 v28, v28\n" \
  "v_exp_f32 v29, v29\n" \
  "v_exp_f32 v30, v30\n" \
  "v_exp_f32 v31, v31\n" \
  "v_exp_f32 v32, v32\n" \
  "v_exp_f32 v33, v33\n" \
  "v_smfmac_f32_32x32x32_bf16 v[0:15], v[180:183], v[24:31], v56\n" \
  "v_exp_f32 v34, v34\n" \
  "v_exp_f32 v35, v35\n" \
  "v_exp_f32 v36, v36\n" \
  "v_mfma_f32_32x32x16_bf16 v[16:31], v[96:99], v[48:51], 0\n" \
  "v_exp_f32 v37, v37\n" \
  "v_exp_f32 v38, v38\n" \
  "v_exp_f32 v39, v39\n" \
  "v_exp_f32 v40, v40\n" \
  "v_exp_f32 v41, v41\n" \
  "v_smfmac_f32_32x32x32_bf16 v[0:15], v[184:187], v[32:39], v56\n" \
  "v_exp_f32 v42, v42\n" \
  "v_exp_f32 v43, v43\n" \
  "v_exp_f32 v44, v44\n" \
  "v_exp_f32 v45, v45\n" \
  "v_exp_f32 v46, v46\n" \
  "v_exp_f32 v47, v47\n" \
  "v_exp_f32 v16, v16\n" \
  "v_exp_f32 v17, v17\n" \
  "v_smfmac_f32_32x32x32_bf16 v[0:15], v[188:191], v[40:47], v56\n" \
  "v_exp_f32 v18, v18\n" \
  "v_exp_f32 v19, v19\n" \
  "v_exp_f32 v20, v20\n" \
  "v_mfma_f32_32x32x16_bf16 v[32:47], v[100:103], v[48:51], 0\n" \
  "v_exp_f32 v21, v21\n" \
  "v_exp_f32 v22, v22\n" \
  "v_exp_f32 v23, v23\n" \
  "v_exp_f32 v24, v24\n" \
  "v_exp_f32 v25, v25\n" \
  "v_smfmac_f32_32x32x32_bf16 v[0:15], v[192:195], v[16:23], v56\n" \
  "v_exp_f32 v26, v26\n" \
  "v_exp_f32 v27, v27\n" \
  "v_exp_f32 v28, v28\n" \
  "v_exp_f32 v29, v29\n" \
  "v_exp_f32 v30, v30\n" \
  "v_exp_f32 v31, v31\n" \
  "v_exp_f32 v32, v32\n" \
  "v_exp_f32 v33, v33\n" \
  "v_smfmac_f32_32x32x32_bf16 v[0:15], v[196:199], v[24:31], v56\n" \
  "v_exp_f32 v34, v34\n" \
  "v_exp_f32 v35, v35\n" \
  "v_exp_f32 v36, v36\n" \
  "v_mfma_f32_32x32x16_bf16 v[16:31], v[104:107], v[48:51], 0\n" \
  "v_exp_f32 v37, v37\n" \
  "v_exp_f32 v38, v38\n" \
  "v_exp_f32 v39, v39\n" \
  "v_exp_f32 v40, v40\n" \
  "v_exp_f32 v41, v41\n" \
  "v_smfmac_f32_32x32x32_bf16 v[0:15], v[200:203], v[32:39], v56\n" \
  "v_exp_f32 v42, v42\n" \
  "v_exp_f32 v43, v43\n" \
  "v_exp_f32 v44, v44\n" \
  "v_exp_f32 v45, v45\n" \
  "v_exp_f32 v46, v46\n" \
  "v_exp_f32 v47, v47\n" \
  "v_exp_f32 v16, v16\n" \
  "v_exp_f32 v17, v17\n" \
  "v_smfmac_f32_32x32x32_bf16 v[0:15], v[204:207], v[40:47], v56\n" \
  "v_exp_f32 v18, v18\n" \
  "v_exp_f32 v19, v19\n" \
  "v_exp_f32 v20, v20\n" \
  "v_mfma_f32_32x32x16_bf16 v[32:47], v[108:111], v[48:51], 0\n" \
  "v_exp_f32 v21, v21\n" \
  "v_exp_f32 v22, v22\n" \
  "v_exp_f32 v23, v23\n" \
  "v_exp_f32 v24, v24\n" \
  "v_exp_f32 v25, v25\n" \
  "v_smfmac_f32_32x32x32_bf16 v[0:15], v[208:211], v[16:23], v56\n" \
  "v_exp_f32 v26, v26\n" \
  "v_exp_f32 v27, v27\n" \
  "v_exp_f32 v28, v28\n" \
  "v_exp_f32 v29, v29\n" \
  "v_exp_f32 v30, v30\n" \
  "v_exp_f32 v31, v31\n" \
  "v_exp_f32 v32, v32\n" \
  "v_exp_f32 v33, v33\n" \
  "v_smfmac_f32_32x32x32_bf16 v[0:15], v[212:215], v[24:31], v56\n" \
  "v_exp_f32 v34, v34\n" \
  "v_exp_f32 v35, v35\n" \
  "v_exp_f32 v36, v36\n" \
  "v_mfma_f32_32x32x16_bf16 v[16:31], v[112:115], v[48:51], 0\n" \
  "v_exp_f32 v37, v37\n" \
  "v_exp_f32 v38, v38\n" \
  "v_exp_f32 v39, v39\n" \
  "v_exp_f32 v40, v40\n" \
  "v_exp_f32 v41, v41\n" \
  "v_smfmac_f32_32x32x32_bf16 v[0:15], v[216:219], v[32:39], v56\n" \
  "v_exp_f32 v42, v42\n" \
  "v_exp_f32 v43, v43\n" \
  "v_exp_f32 v44, v44\n" \
  "v_exp_f32 v45, v45\n" \
  "v_exp_f32 v46, v46\n" \
  "v_exp_f32 v47, v47\n" \
  "v_exp_f32 v16, v16\n" \
  "v_exp_f32 v17, v17\n" \
  "v_smfmac_f32_32x32x32_bf16 v[0:15], v[220:223], v[40:47], v56\n" \
  "v_exp_f32 v18, v18\n" \
  "v_exp_f32 v19, v19\n" \
  "v_exp_f32 v20, v20\n" \
  "v_mfma_f32_32x32x16_bf16 v[32:47], v[116:119], v[48:51], 0\n" \
  "v_exp_f32 v21, v21\n" \
  "v_exp_f32 v22, v22\n" \
  "v_exp_f32 v23, v23\n" \
  "v_exp_f32 v24, v24\n" \
  "v_exp_f32 v25, v25\n" \
  "v_smfmac_f32_32x32x32_bf16 v[0:15], v[224:227], v[16:23], v56\n" \
  "v_exp_f32 v26, v26\n" \
  "v_exp_f32 v27, v27\n" \
  "v_exp_f32 v28, v28\n" \
  "v_exp_f32 v29, v29\n" \
  "v_exp_f32 v30, v30\n" \
  "v_exp_f32 v31, v31\n" \
  "v_exp_f32 v32, v32\n" \
  "v_exp_f32 v33, v33\n" \
  "v_smfmac_f32_32x32x32_bf16 v[0:15], v[228:231], v[24:31], v56\n" \
  "v_exp_f32 v34, v34\n" \
  "v_exp_f32 v35, v35\n" \
  "v_exp_f32 v36, v36\n" \
  "v_mfma_f32_32x32x16_bf16 v[16:31], v[120:123], v[48:51], 0\n" \
  "v_exp_f32 v37, v37\n" \
  "v_exp_f32 v38, v38\n" \
  "v_exp_f32 v39, v39\n" \
  "v_exp_f32 v40, v40\n" \
  "v_exp_f32 v41, v41\n" \
  "v_smfmac_f32_32x32x32_bf16 v[0:15], v[232:235], v[32:39], v56\n" \
  "v_exp_f32 v42, v42\n" \
  "v_exp_f32 v43, v43\n" \
  "v_exp_f32 v44, v44\n" \
  "v_exp_f32 v45, v45\n" \
  "v_exp_f32 v46, v46\n" \
  "v_exp_f32 v47, v47\n" \
  "v_exp_f32 v16, v16\n" \
  "v_exp_f32 v17, v17\n" \
  "v_smfmac_f32_32x32x32_bf16 v[0:15], v[236:239], v[40:47], v56\n" \
  "v_exp_f32 v18, v18\n" \
  "v_exp_f32 v19, v19\n" \
  "v_exp_f32 v20, v20\n" \
  "v_mfma_f32_32x32x16_bf16 v[32:47], v[124:127], v[48:51], 0\n" \
  "v_exp_f32 v21, v21\n" \
  "v_exp_f32 v22, v22\n" \
  "v_exp_f32 v23, v23\n" \
  "v_exp_f32 v24, v24\n" \
  "v_exp_f32 v25, v25\n" \
  "v_smfmac_f32_32x32x32_bf16 v[0:15], v[240:243], v[16:23], v56\n" \
  "v_exp_f32 v26, v26\n" \
  "v_exp_f32 v27, v27\n" \
  "v_exp_f32 v28, v28\n" \
  "v_exp_f32 v29, v29\n" \
  "v_exp_f32 v30, v30\n" \
  "v_exp_f32 v31, v31\n" \
  "v_exp_f32 v32, v32\n" \
  "v_exp_f32 v33, v33\n" \
  "v_smfmac_f32_32x32x32_bf16 v[0:15], v[244:247], v[24:31], v56\n" \
  "v_exp_f32 v34, v34\n" \
  "v_exp_f32 v35, v35\n" \
  "v_exp_f32 v36, v36\n" \
  "s_waitcnt lgkmcnt(0)\n" \
  "v_mfma_f32_32x32x16_bf16 v[16:31], v[64:67], v[52:55], 0\n" \
  "v_mov_b64 v[48:49], v[52:53]\n" \
  "v_mov_b64 v[50:51], v[54:55]\n" \
  "v_exp_f32 v37, v37\n" \
  "v_exp_f32 v38, v38\n" \
  "v_exp_f32 v39, v39\n" \
  "v_exp_f32 v40, v40\n" \
  "v_exp_f32 v41, v41\n" \
  "v_smfmac_f32_32x32x32_bf16 v[0:15], v[248:251], v[32:39], v56\n" \
  "v_exp_f32 v42, v42\n" \
  "v_exp_f32 v43, v43\n" \
  "v_exp_f32 v44, v44\n" \
  "v_exp_f32 v45, v45\n" \
  "v_exp_f32 v46, v46\n" \
  "v_exp_f32 v47, v47\n" \
  "s_sub_u32 s48, s48, 1\n" \
  "s_cmp_lg_u32 s48, 0\n" \
  "s_cbranch_scc1 Lcw_loop%=\n" \
  "v_smfmac_f32_32x32x32_bf16 v[0:15], v[252:255], v[40:47], v56\n" \
  "ds_read_b64 v[60:61], v58\n" \
  "s_nop 7\n" \
  "s_nop 3\n" \
  "Lcw_chk2_%=:\n" \
  "s_waitcnt lgkmcnt(0)\n" \
  "v_and_b32 v60, v60, v61\n" \
  "v_cmp_eq_u32 vcc, 0, v60\n" \
  "s_cbranch_vccnz Lcw_spin2_%=\n" \
  "ds_write_b128 v59, v[0:3]\n" \
  "ds_write_b128 v59, v[4:7] offset:1024\n" \
  "ds_write_b32 v59, v8 offset:2048\n" \
  "ds_write_b32 v62, v56\n" \
  "s_branch Lcw_end%=\n" \
  "Lcw_spin0_%=:\n" \
  "s_sleep 2\n" \
  "ds_read_b64 v[60:61], v58\n" \
  "s_branch Lcw_chk0_%=\n" \
  "Lcw_spin1_%=:\n" \
  "s_sleep 2\n" \
  "ds_read_b64 v[60:61], v58\n" \
  "s_branch Lcw_chk1_%=\n" \
  "Lcw_spin2_%=:\n" \
  "s_sleep 2\n" \
  "ds_read_b64 v[60:61], v58\n" \
  "s_branch Lcw_chk2_%=\n" \
  "Lcw_end%=:\n" \
  "s_waitcnt vmcnt(0) lgkmcnt(0)\n"
#define COMPUTE_CLOBBERS "v0","v1","v2","v3","v4","v5","v6","v7","v8","v9","v10","v11","v12","v13","v14","v15","v16","v17","v18","v19","v20","v21","v22","v23","v24","v25","v26","v27","v28","v29","v30","v31","v32","v33","v34","v35","v36","v37","v38","v39","v40","v41","v42","v43","v44","v45","v46","v47","v48","v49","v50","v51","v52","v53","v54","v55","v56","v57","v58","v59","v60","v61","v62","v63","v64","v65","v66","v67","v68","v69","v70","v71","v72","v73","v74","v75","v76","v77","v78","v79","v80","v81","v82","v83","v84","v85","v86","v87","v88","v89","v90","v91","v92","v93","v94","v95","v96","v97","v98","v99","v100","v101","v102","v103","v104","v105","v106","v107","v108","v109","v110","v111","v112","v113","v114","v115","v116","v117","v118","v119","v120","v121","v122","v123","v124","v125","v126","v127","v128","v129","v130","v131","v132","v133","v134","v135","v136","v137","v138","v139","v140","v141","v142","v143","v144","v145","v146","v147","v148","v149","v150","v151","v152","v153","v154","v155","v156","v157","v158","v159","v160","v161","v162","v163","v164","v165","v166","v167","v168","v169","v170","v171","v172","v173","v174","v175","v176","v177","v178","v179","v180","v181","v182","v183","v184","v185","v186","v187","v188","v189","v190","v191","v192","v193","v194","v195","v196","v197","v198","v199","v200","v201","v202","v203","v204","v205","v206","v207","v208","v209","v210","v211","v212","v213","v214","v215","v216","v217","v218","v219","v220","v221","v222","v223","v224","v225","v226","v227","v228","v229","v230","v231","v232","v233","v234","v235","v236","v237","v238","v239","v240","v241","v242","v243","v244","v245","v246","v247","v248","v249","v250","v251","v252","v253","v254","v255","s40","s41","s44","s45","s46","s47","s48","s49","vcc","scc","memory"

constexpr int L_XI = 0;
constexpr int L_OB = 0;
constexpr int L_QX = 32768;
constexpr int L_P1KV = 65536;
constexpr int L_SLOT = 69632;
constexpr int L_STG = 81920;
constexpr int L_QI = 147456;
constexpr int L_S1 = 155648;
constexpr int L_S2 = L_S1 + 1024;
constexpr int L_CS = L_S2 + 1024;
constexpr int L_XS = L_CS + 1024;
constexpr int L_HS = L_XS + 3072;
constexpr int L_CTRL = L_HS + 1024;
constexpr int L_YS = L_S1;
constexpr int L_PF = L_CTRL + 512;
constexpr int L_TOTAL = L_PF + 256;
static_assert(L_TOTAL <= 163840, "LDS");

DI f32x8 lo8(const f32x16& s) { return __builtin_shufflevector(s, s, 0, 1, 2, 3, 4, 5, 6, 7); }
DI f32x8 hi8(const f32x16& s) { return __builtin_shufflevector(s, s, 8, 9, 10, 11, 12, 13, 14, 15); }
DI float silu_f(float v) { return v * __builtin_amdgcn_rcpf(1.0f + __builtin_amdgcn_exp2f(-1.4426950408889634f * v)); }
DI unsigned lds_ld(const char* p) { return __hip_atomic_load((LAS unsigned*)p, __ATOMIC_RELAXED, __HIP_MEMORY_SCOPE_WORKGROUP); }
DI void lds_st(char* p, unsigned v) { asm volatile("" ::: "memory"); __hip_atomic_store((LAS unsigned*)p, v, __ATOMIC_RELAXED, __HIP_MEMORY_SCOPE_WORKGROUP); }
DI unsigned long long gld64(const char* p) { return __hip_atomic_load((gu64*)p, RLX_AGENT); }
DI void gst64(char* p, unsigned v, unsigned tag) { __hip_atomic_store((gu64*)p, ((unsigned long long)tag << 32) | v, RLX_AGENT); }
constexpr unsigned SPIN_CAP = 1u << 18;
constexpr float QSCALE = 0.25f * 1.4426950408889634f;

__global__ __launch_bounds__(512, 2) void fused_mqa_mod(const char* __restrict__ x, const char* __restrict__ wq, const char* __restrict__ wk,
                                                        const char* __restrict__ wv, const char* __restrict__ wo, const char* __restrict__ wr,
                                                        const char* __restrict__ wg, const char* __restrict__ wu, const char* __restrict__ wd,
                                                        char* ws, char* __restrict__ out) {
  extern __shared__ __attribute__((aligned(16))) char smem[];
  const int tid = threadIdx.x, lane = tid & 63, w = __builtin_amdgcn_readfirstlane(tid >> 6);
  const int b = blockIdx.x, xcd = b & 7, idx = b >> 3, tt = xcd * 8 + (idx >> 2), sib = idx & 3, tok0 = tt * 32;
  const unsigned lds0 = (unsigned)(size_t)smem;
  const int r = lane & 31, h = lane >> 5, n = lane & 15, g4 = lane >> 4, q4 = n >> 2, p4 = n & 3;
  const int slab = sib >> 1, which = sib & 1;
  const int hw = w & 3;
  const unsigned ONE2 = 0x3f803f80u;
  const u32x4 ones4 = {ONE2, ONE2, ONE2, ONE2};
  f32x16 zero; for (int i = 0; i < 16; ++i) zero[i] = 0.f;

  asm volatile("" :: "s"(x), "s"(wq), "s"(wk), "s"(wv), "s"(wo), "s"(wr), "s"(wg), "s"(wu), "s"(wd), "s"(ws), "s"(out));
  unsigned tag;
  const char* xa = smem + L_XI + r * 128;
  const int xsw = (r >> 1) & 7;
  f32x2 fs; float mi;
  u32x4 x0, x1;
  unsigned gp[8];
  const int ecol = 128 * sib + 32 * hw + 16 * h;
  auto row_stats = [&](float& m, float& sq) {
    const f32x4 t1 = *(const f32x4*)(smem + L_S1 + r * 16), t2 = *(const f32x4*)(smem + L_S2 + r * 16);
    ln_stats((t1[0] + t1[1]) + (t1[2] + t1[3]), (t2[0] + t2[1]) + (t2[2] + t2[3]), m, sq);
  };
  auto p1_wait = [&](unsigned level) {
    while (!__all(lds_ld(smem + L_CTRL + 340 + (lane & 3) * 4) >= level)) __builtin_amdgcn_s_sleep(1);
    asm volatile("" ::: "memory");
  };
  if (__builtin_expect(w < 4, 1)) {
    if (lane == 0) {
      glds4c(ws + WS_EPOCH, lds0 + L_HS + w * 4);
    }
    {
      const int row = lane >> 3;
      const char* xs = x + (size_t)(tok0 + row) * 1024 + w * 256;
#pragma unroll
      for (int i = 0; i < 8; ++i)
        glds16(xs + (i >> 2) * 128 + (i & 3) * 8192 + (((lane & 7) ^ (((8 * (i & 3) + row) >> 1) & 7)) << 4), lds0 + L_XI + w * 8192 + i * 1024);
      const char* wkv = (which ? wv : wk) + w * 4096 + lane * 16;
#pragma unroll
      for (int i = 0; i < 4; ++i) glds16(wkv + i * 1024, lds0 + L_P1KV + w * 4096 + i * 1024);
    }
    if (w == 1) glds16(wr + lane * 16, lds0 + L_XS);
    __builtin_amdgcn_s_setprio(3);
    __builtin_amdgcn_s_barrier();
    const int kr = slab * 16 + n;
    unsigned a_kx0 = L_XI + w * 8192 + kr * 128 + ((g4 ^ ((kr >> 1) & 7)) << 4), a_kx1 = L_XI + w * 8192 + kr * 128 + (((4 + g4) ^ ((kr >> 1) & 7)) << 4);
    unsigned a_kw = L_P1KV + w * 4096 + (8 * g4 + q4) * 32 + 8 * p4, a_pw = L_P1KV + w * 4096 + lane * 16, a_pr = L_P1KV + lane * 16;
    unsigned a_fl = L_CTRL + 340 + (lane & 3) * 4;
    char* kvdst = ws + WS_KV + (size_t)tt * 2048 + (which == 0 ? ((g4 & 1) * 32 + slab * 16 + n) * 16 + (g4 >> 1) * 8 : 1024 + slab * 512 + ((g4 >> 1) * 16 + n) * 16 + (g4 & 1) * 8);
    char* fldst = ws + WS_KVF + (size_t)((lane & 7) * 1024 + tt * 16 + sib) * 4;
    u32x4 on = ones4;
    asm volatile("" : "+v"(a_kx0), "+v"(a_kx1), "+v"(a_kw), "+v"(a_pw), "+v"(a_pr), "+v"(a_fl), "+v"(kvdst), "+v"(fldst), "+v"(on));
    VMWAIT(0);
    bf16x8 kx[4], kw[4];
#pragma unroll
    for (int k2 = 0; k2 < 4; ++k2) {
      kx[k2] = *(const bf16x8*)(smem + ((k2 & 1) ? a_kx1 : a_kx0) + (k2 >> 1) * 4096);
      kw[k2] = cat(trread(smem + a_kw + k2 * 1024), trread(smem + a_kw + k2 * 1024 + 128));
    }
    const unsigned ep = lds_ld(smem + L_HS + w * 4);
    asm volatile("" ::: "memory");
    f32x4 kacc = {0.f, 0.f, 0.f, 0.f}, kcs = kacc, krs = kacc;
    float q2a = 0.f, q2b = 0.f;
    if (which == 0) {
#pragma unroll
      for (int k2 = 0; k2 < 4; ++k2) { kacc = MFMA16(kw[k2], kx[k2], kacc); kcs = MFMA16(kw[k2], BC(bf16x8, on), kcs); krs = MFMA16(BC(bf16x8, on), kx[k2], krs); }
    } else {
#pragma unroll
      for (int k2 = 0; k2 < 4; ++k2) { kacc = MFMA16(kx[k2], kw[k2], kacc); kcs = MFMA16(BC(bf16x8, on), kw[k2], kcs); krs = MFMA16(BC(bf16x8, on), kx[k2], krs); }
    }
#pragma unroll
    for (int k2 = 0; k2 < 4; ++k2) {
      const u32x4 u = BC(u32x4, kx[k2]);
      q2a = dot2(u[0], u[0], q2a); q2b = dot2(u[1], u[1], q2b); q2a = dot2(u[2], u[2], q2a); q2b = dot2(u[3], u[3], q2b);
    }
    *(f32x4*)(smem + a_pw) = kacc;
    *(f32x4*)(smem + a_pw + 1024) = kcs;
    *(f32x2*)(smem + a_pw + 2048) = f32x2{krs[0], q2a + q2b};
    lds_st(smem + L_CTRL + 340 + w * 4, 1u);
    tag = __builtin_amdgcn_readfirstlane(ep) + 1u;
    if (__builtin_expect(w == 0, 1)) {
      while (!__all(lds_ld(smem + a_fl) != 0u)) {}
      asm volatile("" ::: "memory");
      f32x4 a = {0.f, 0.f, 0.f, 0.f}, c = a;
      f32x2 st = {0.f, 0.f};
#pragma unroll
      for (int i = 0; i < 4; ++i) st += *(const f32x2*)(smem + a_pr + i * 4096 + 2048);
#pragma unroll
      for (int i = 0; i < 4; ++i) { a += *(const f32x4*)(smem + a_pr + i * 4096); c += *(const f32x4*)(smem + a_pr + i * 4096 + 1024); }
      float m, sq; ln_stats(st[0], halfsum(x16sum(st[1])), m, sq);
      const float inv = __builtin_amdgcn_rcpf(sq);
      float v[4];
      if (which == 0) {
#pragma unroll
        for (int e = 0; e < 4; ++e) v[e] = (a[e] - m * c[e]) * inv;
      } else {
#pragma unroll
        for (int e = 0; e < 4; ++e) v[e] = (a[e] - __shfl(m, 4 * g4 + e) * c[e]) * __shfl(inv, 4 * g4 + e);
      }
      gst64(kvdst, pk2(v[0], v[1]), pk2(v[2], v[3]));
      lds_st(smem + L_CTRL + 28, 1u);
    }
    {
      u32x4 xf[8];
#pragma unroll
      for (int ks = 0; ks < 8; ++ks) xf[ks] = *(const u32x4*)(xa + w * 8192 + (ks >> 2) * 4096 + (((2 * (ks & 3) + h) ^ xsw) << 4));
      float s1a = 0.f, s1b = 0.f, s2a = 0.f, s2b = 0.f;
#pragma unroll
      for (int ks = 0; ks < 8; ++ks) {
        s1a = dot2(xf[ks][0], ONE2, s1a); s2a = dot2(xf[ks][0], xf[ks][0], s2a); s1b = dot2(xf[ks][1], ONE2, s1b); s2b = dot2(xf[ks][1], xf[ks][1], s2b);
        s1a = dot2(xf[ks][2], ONE2, s1a); s2a = dot2(xf[ks][2], xf[ks][2], s2a); s1b = dot2(xf[ks][3], ONE2, s1b); s2b = dot2(xf[ks][3], xf[ks][3], s2b);
      }
      const float s1 = halfsum(s1a + s1b), s2 = halfsum(s2a + s2b);
      *(float*)(smem + (h ? L_S2 : L_S1) + r * 16 + w * 4) = h ? s2 : s1;
      lds_st(smem + L_CTRL + 340 + w * 4, 2u);
    }
    if (w == 0) {
      VMWAIT(0);
      if (lane < 8) __hip_atomic_store((gu32*)fldst, tag, RLX_AGENT);
      lds_st(smem + L_CTRL + 24, 1u);
      glds4(wo + (size_t)(idx * 64 + lane) * 128, lds0 + L_PF);
      glds4(wo + (size_t)(2048 + idx * 64 + lane) * 128, lds0 + L_PF);
    }
    if (w == 1) {
    p1_wait(2);
    const int row = sib * 8 + (n & 7);
    const u32x4 wl = *(const u32x4*)(smem + L_XS + lane * 16);
    f32x4 d = {0.f, 0.f, 0.f, 0.f};
#pragma unroll
    for (int ks = 0; ks < 16; ++ks)
      d = MFMA16(*(const bf16x8*)(smem + L_XI + (ks >> 1) * 4096 + row * 128 + (((4 * (ks & 1) + g4) ^ ((row >> 1) & 7)) << 4)),
                 *(const bf16x8*)(smem + L_XS + ks * 64 + g4 * 16), d);
    const float sw = wavesum(dot2(wl[0], ONE2, dot2(wl[1], ONE2, dot2(wl[2], ONE2, dot2(wl[3], ONE2, 0.f)))));
    const int r0 = sib * 8 + 4 * (g4 & 1);
    float m, sq; row_stats(m, sq);
    const float inv = __builtin_amdgcn_rcpf(sq);
    if ((lane >> 3) == sib) gst64(ws + WS_STG + (size_t)(tok0 + lane) * 8, (BC(unsigned, m) >> 16) | (BC(unsigned, sq) & 0xffff0000u), tag);
    unsigned k[4];
#pragma unroll
    for (int e = 0; e < 4; ++e) {
      const float mm = __shfl(m, r0 + e), ii = __shfl(inv, r0 + e);
      unsigned u = BC(unsigned, (d[e] - mm * sw) * ii); u = (u & 0x80000000u) ? ~u : (u | 0x80000000u);
      k[e] = (u & 0xfffff800u) | (unsigned)(2047 - (tok0 + r0 + e));
    }
    unsigned a1 = max(k[0], k[1]), a2 = min(k[0], k[1]), b1 = max(k[2], k[3]), b2 = min(k[2], k[3]);
    a2 = max(min(a1, b1), max(a2, b2)); a1 = max(a1, b1);
    b1 = __shfl_xor(a1, 16); b2 = __shfl_xor(a2, 16);
    a2 = max(min(a1, b1), max(a2, b2)); a1 = max(a1, b1);
    if (lane < 2) gst64(ws + WS_KEYG + (size_t)(tt * 4 + sib) * 16 + lane * 8, lane ? a2 : a1, tag);
    lds_st(smem + L_CTRL + 60, 1u);
    }
  } else {
    if (hw == 0) *(u32x2*)(smem + L_CTRL + lane * 8) = u32x2{0u, (lane & 3) == 2 && lane >= 12 && lane < 40 ? 1u : 0u};
    if (hw == 1) *(u32x4*)(smem + L_XS + 2048 + lane * 16) = ones4;
    const char* wqs = wq + (size_t)(lane >> 2) * 1024 + sib * 256 + hw * 64 + (lane & 3) * 16;
    BARRIER();
    __builtin_amdgcn_s_setprio(2);
    if (hw == 3 && lane < 18) {
      const int i = lane;
      const size_t o = i < 8 ? WS_KVF + (size_t)i * 4096 + tt * 64
                     : i < 12 ? WS_KV + (size_t)tt * 2048 + (which ? 1024 + slab * 512 + (i & 3) * 128 : slab * 256 + (i & 1) * 128 + (i & 2) * 256)
                     : i == 12 ? WS_KEYG + (size_t)(tt * 4 + sib) * 16 : i == 13 ? WS_STG + (size_t)(tok0 + sib * 8) * 8
                     : i == 14 ? WS_HG + (size_t)b * 64 : WS_YG + (size_t)(((idx >> 2) * 2 + (i & 1)) * 512 + (xcd * 4 + sib) * 16) * 8;
      glds4c(ws + o, lds0 + L_PF);
    }
    if (hw == 1 || hw == 2) glds4(wq + (size_t)((hw - 1) * 2048 + idx * 64 + lane) * 128, lds0 + L_PF);
    if (hw) { while (!lds_ld(smem + L_CTRL + 28)) __builtin_amdgcn_s_sleep(1); }
    u32x4 w2[8];
#pragma unroll
    for (int i = 0; i < 8; ++i) w2[i] = *(const u32x4*)(wqs + (24 + i) * 16384);
    asm volatile("" ::: "memory");
#pragma unroll
    for (int i = 0; i < 24; ++i) {
      glds16(wqs + i * 16384, lds0 + (i < 16 ? L_STG + hw * 16384 + i * 1024 : L_QX + (3 - hw) * 8192 + (i - 16) * 1024));
    }
    asm volatile("" ::: "memory");
    p1_wait(1);
    tag = __builtin_amdgcn_readfirstlane(lds_ld(smem + L_HS)) + 1u;
    float m, sq;
    auto stats = [&]() { p1_wait(2); row_stats(m, sq); fs = f32x2{m, __builtin_amdgcn_rcpf(sq)}; mi = -fs[0] * fs[1]; };
    auto gate = [&]() {
    x0 = *(const u32x4*)(xa + (ecol >> 6) * 4096 + ((((ecol & 63) >> 3) ^ xsw) << 4)); x1 = *(const u32x4*)(xa + (ecol >> 6) * 4096 + (((((ecol & 63) >> 3) + 1) ^ xsw) << 4));
#pragma unroll
  for (int e = 0; e < 8; ++e) { const unsigned u = e < 4 ? x0[e] : x1[e - 4]; gp[e] = pk2(silu_f(bflo(u) * fs[1] + mi), silu_f(bfhi(u) * fs[1] + mi)); }
  asm volatile("" : "+v"(x0), "+v"(x1), "+v"(gp[0]), "+v"(gp[1]), "+v"(gp[2]), "+v"(gp[3]), "+v"(gp[4]), "+v"(gp[5]), "+v"(gp[6]), "+v"(gp[7]));
    };
    if (hw) { stats(); gate(); }
    f32x16 qacc = zero;
    float csa = 0.f, csb = 0.f;
    const int wo_ = (8 * h + q4) * 64 + (g4 & 1) * 32 + 8 * p4;
    bf16x8 wf[2][8], xb[2][8];
    auto q_read = [&](auto RD) {
      constexpr int rd = decltype(RD)::value;
      if (rd == 0) VMWAIT(16); else if (rd == 1) VMWAIT(8); else if (rd == 2) VMWAIT(0); else asm volatile("" ::: "memory");
      const char* wa = smem + (rd == 2 ? L_QX + (3 - hw) * 8192 : L_STG + hw * 16384 + (rd == 1 ? 8192 : 0)) + wo_;
#pragma unroll
      for (int ks = 0; ks < 8; ++ks) {
        wf[rd & 1][ks] = cat(trread(wa + ks * 1024), trread(wa + ks * 1024 + 256));
        xb[rd & 1][ks] = *(const bf16x8*)(xa + (rd * 2 + (ks >> 2)) * 4096 + (((2 * (ks & 3) + h) ^ xsw) << 4));
      }
      if (rd == 2) {
#pragma unroll
        for (int ks = 0; ks < 8; ++ks) *(u32x4*)(smem + L_STG + hw * 16384 + ks * 1024 + lane * 16) = w2[ks];
      }
      SB;
    };
    q_read(IC<0>{});
    static_for<0, 4>([&](auto RD) {
      constexpr int rd = decltype(RD)::value;
      if constexpr (rd < 3) q_read(IC<rd + 1>{});
#pragma unroll
      for (int ks = 0; ks < 8; ++ks) {
        qacc = MFMA32(wf[rd & 1][ks], xb[rd & 1][ks], qacc);
        const u32x4 wu4 = BC(u32x4, wf[rd & 1][ks]);
        csa = dot2(wu4[0], ONE2, csa); csb = dot2(wu4[1], ONE2, csb); csa = dot2(wu4[2], ONE2, csa); csb = dot2(wu4[3], ONE2, csb);
      }
      asm volatile("" : "+v"(csa), "+v"(csb), "+v"(qacc));
      SB;
    });
    if (hw == 0) stats();
    const float cs = halfsum(csa + csb);
    if (h == 0) *(float*)(smem + L_CS + hw * 128 + r * 4) = cs;
    asm volatile("" ::: "memory");
    const float sc = fs[1] * QSCALE;
#pragma unroll
    for (int hh = 0; hh < 2; ++hh) {
      u32x4 o;
#pragma unroll
      for (int g = 0; g < 2; ++g) {
        const f32x4 c = *(const f32x4*)(smem + L_CS + hw * 128 + (16 * hh + 8 * g + 4 * h) * 4);
        const int i0 = 8 * hh + 4 * g;
        o[2 * g] = pk2((qacc[i0] - m * c[0]) * sc, (qacc[i0 + 1] - m * c[1]) * sc);
        o[2 * g + 1] = pk2((qacc[i0 + 2] - m * c[2]) * sc, (qacc[i0 + 3] - m * c[3]) * sc);
      }
      *(u32x4*)(smem + L_QI + (2 * hw + hh) * 1024 + lane * 16) = o;
    }
    lds_st(smem + L_CTRL + (hw == 0 ? 56 : hw == 1 ? 80 : hw == 2 ? 84 : 112), 1u);
    if (hw == 0) gate();
    lds_st(smem + L_CTRL + 356 + hw * 4, 1u);
  }
  tag = __builtin_amdgcn_readfirstlane(tag);
  asm volatile("" : "+s"(tag));
  const __amdgpu_buffer_rsrc_t rs = __builtin_amdgcn_make_buffer_rsrc(ws, 0, 8u << 20, 0x00020000);
  char* ctrl = smem + L_CTRL;
  auto merge = [&](int j) {
    const int lane = __builtin_amdgcn_mbcnt_hi(-1, __builtin_amdgcn_mbcnt_lo(-1, 0)), r = lane & 31, h = lane >> 5;
    const int ps = j + 2;
    const char* pb = smem + L_SLOT - (ps & 3) * 12288 + lane * 16;
    f32x4 a = *(const f32x4*)(pb), c = *(const f32x4*)(pb + 1024);
    float l = *(const float*)(pb + 2048);
#pragma unroll
    for (int p = 1; p < 4; ++p) { a += *(const f32x4*)(pb + p * 3072); c += *(const f32x4*)(pb + p * 3072 + 1024); l += *(const float*)(pb + p * 3072 + 2048); }
    if (j < 2) {
#pragma unroll
      for (int p = 0; p < 4; ++p) { a += *(const f32x4*)(pb + 24576 + p * 3072); c += *(const f32x4*)(pb + 24576 + p * 3072 + 1024); l += *(const float*)(pb + 24576 + p * 3072 + 2048); }
    }
    float il = __builtin_amdgcn_rcpf(l);
    a *= il; c *= il;
    const float am = ((fabsf(a[0]) + fabsf(a[1])) + (fabsf(a[2]) + fabsf(a[3]))) + ((fabsf(c[0]) + fabsf(c[1])) + (fabsf(c[2]) + fabsf(c[3])));
    const bool ok = __all((l >= 1.0e-15f) && (l <= 1.0e30f) && (am <= 1.0e30f));
    if (!ok) {
      const bf16x8 q = *(const bf16x8*)(smem + L_QI + j * 1024 + lane * 16);
      const unsigned cv = (r == 16 || r == 20) ? ONE2 : 0u;
      const u32x4 cv4 = {cv, cv, cv, cv};
      f32x16 o = zero;
      float mrow = -3.0e38f;
#pragma unroll 1
      for (int t = 0; t < 64; ++t) {
        const int rec = (int)WS_KV + t * 2048;
        const bf16x8 kf = BC(bf16x8, __builtin_amdgcn_raw_buffer_load_b128(rs, lane * 16, rec, 16));
        u32x4 v0 = BC(u32x4, __builtin_amdgcn_raw_buffer_load_b128(rs, (h * 16 + (r & 15)) * 16, rec + 1024, 16));
        u32x4 v1 = BC(u32x4, __builtin_amdgcn_raw_buffer_load_b128(rs, (h * 16 + (r & 15)) * 16, rec + 1536, 16));
        if (r >= 16) { v0 = cv4; v1 = cv4; }
        f32x16 s = MFMA32(kf, q, zero);
        float mx = s[0];
#pragma unroll
        for (int i = 1; i < 16; ++i) mx = fmaxf(mx, s[i]);
        mx = fmaxf(halfmax(mx), mrow);
        const float alpha = __builtin_amdgcn_exp2f(mrow - mx);
        mrow = mx;
#pragma unroll
        for (int i = 0; i < 9; ++i) o[i] *= alpha;
#pragma unroll
        for (int i = 0; i < 16; ++i) s[i] = __builtin_amdgcn_exp2f(s[i] - mx);
        o = SMF(BC(bf16x8, v0), lo8(s), o);
        o = SMF(BC(bf16x8, v1), hi8(s), o);
      }
      il = __builtin_amdgcn_rcpf(o[8]);
      a = f32x4{o[0], o[1], o[2], o[3]} * il; c = f32x4{o[4], o[5], o[6], o[7]} * il;
    }
    const unsigned p0 = pk2(a[0], a[1]), p1 = pk2(a[2], a[3]), p2 = pk2(c[0], c[1]), p3 = pk2(c[2], c[3]);
    const int go = (int)WS_OX + ((tt * 8 + j) * 4 + sib) * 2048;
    if (j == 7) {
      __builtin_amdgcn_raw_buffer_store_b128(u32x4{p0, tag, p1, tag}, rs, lane * 16, go, 16);
      __builtin_amdgcn_raw_buffer_store_b128(u32x4{p2, tag, p3, tag}, rs, lane * 16, go + 1024, 16);
    }
    *(u32x4*)(smem + L_OB + (j * 4 + sib) * 1024 + lane * 16) = u32x4{p0, p1, p2, p3};
    lds_st(ctrl + 384 + (j * 4 + sib) * 4, 1u);
    if (j < 4) { lds_st(ctrl + (j < 2 ? j + 4 : j + 6) * 32 + 16, 1u); if (j < 2) lds_st(ctrl + (j + 6) * 32 + 16, 1u); }
    if (j < 7) {
      asm volatile("" ::: "memory");
      __builtin_amdgcn_raw_buffer_store_b128(u32x4{p0, tag, p1, tag}, rs, lane * 16, go, 16);
      __builtin_amdgcn_raw_buffer_store_b128(u32x4{p2, tag, p3, tag}, rs, lane * 16, go + 1024, 16);
    }
  };
  if (w < 4) {
    __builtin_amdgcn_s_setprio(0);
    const char* kvb = ws + WS_KV + (size_t)w * 32768;
    const char* fb = ws + WS_KVF + xcd * 4096 + w * 1024;
    const int bq = b < 224 ? b : 0;
    const char* pfb = w < 2 ? (w ? wu : wg) + (size_t)(bq & 7) * 64 * (F_ * 2) + (bq >> 3) * 128
                    : w == 2 ? wd + (size_t)(idx < 28 ? idx : 27) * 65536 + xcd * 128 : ws + WS_PG + (size_t)b * 2048;
    const int pfs = w < 2 ? F_ * 2 : w == 2 ? 1024 : 32;
    asm volatile(COMPUTE_ASM :: "s"(kvb), "s"(fb), "s"(tag), "s"(lds0 + L_SLOT + w * 3072), "s"(lds0 + L_QI), "s"(lds0 + L_CTRL + w * 4), "s"(lds0 + L_CTRL + 24), "s"(pfb), "s"(pfs) : COMPUTE_CLOBBERS);
    asm volatile("" : "+s"(tag));
    if (w == 0) {
      const int l3 = __builtin_amdgcn_mbcnt_lo(-1, 0) & 3;
      while (!__all(lds_ld(ctrl + 9 * 32 + l3 * 4) != 0u)) {}
      asm volatile("" ::: "memory");
      merge(7);
    }
    return;
  }
  __builtin_amdgcn_s_setprio(3);
  const bool ffa = b < 224;
  const int cg = b >> 3, kp = b & 7;
  const int fr = idx >> 2, cc = xcd * 4 + (idx & 3);
  const char* stg = smem + L_STG + hw * 16384;
  const int role = hw;
  const bool fetch = hw != 0;
  const int fs_ = (sib + hw) & 3;

  float sm = 0.f, si = 0.f;
  auto scan = [&]() {
  if (role == 0) {
    unsigned a1, a2;
    for (unsigned spins = 0; spins < SPIN_CAP; ++spins) {
      unsigned long long kg[8]; bool ok = true; a1 = 0; a2 = 0;
#pragma unroll
      for (int i = 0; i < 8; ++i) kg[i] = gld64(ws + WS_KEYG + (i * 64 + lane) * 8);
#pragma unroll
      for (int i = 0; i < 8; ++i) { ok &= (unsigned)(kg[i] >> 32) == tag; const unsigned kk = (unsigned)kg[i]; a2 = max(a2, min(a1, kk)); a1 = max(a1, kk); }
      if (__all(ok)) break;
      __builtin_amdgcn_s_sleep(8);
    }
    const unsigned m1 = wavemaxu(a1), m2 = wavemaxu(a1 == m1 ? a2 : a1);
    unsigned k = (lane & 1) ? m2 : m1;
    const int sel = 2047 - (int)(k & 2047u);
    k &= 0xfffff800u; k = (k & 0x80000000u) ? (k & 0x7fffffffu) : ~k;
    const float rsv = rbf(__builtin_amdgcn_rcpf(1.0f + __builtin_amdgcn_exp2f(-1.4426950408889634f * rbf(BC(float, k)))));
    if (lane < 2) { *(int*)(ctrl + 320 + lane * 4) = sel; *(float*)(ctrl + 328 + lane * 4) = rsv; }
    lds_st(ctrl + 336, 1u);
    glds16(x + (size_t)(2047 - (int)(m1 & 2047u)) * 1024 + lane * 16, lds0 + L_XS);
    glds16(x + (size_t)(2047 - (int)(m2 & 2047u)) * 1024 + lane * 16, lds0 + L_XS + 1024);
    unsigned long long sg = gld64(ws + WS_STG + (size_t)sel * 8);
    for (unsigned spins = 0; spins < SPIN_CAP; ++spins) {
      if (spins) sg = gld64(ws + WS_STG + (size_t)sel * 8);
      if (__all((unsigned)(sg >> 32) == tag)) break;
      __builtin_amdgcn_s_sleep(8);
    }
    sm = bflo((unsigned)sg); si = __builtin_amdgcn_rcpf(bfhi((unsigned)sg));
  }
  };
  if (role) { while (!__all(lds_ld(ctrl + (lane & 3) * 4) != 0u)) __builtin_amdgcn_s_sleep(4); }
  bf16x8 wof[32];
  const unsigned wvo = (lane >> 2) * 1024 + (lane & 3) * 16;
  auto wo_stage = [&](int hf) {
#pragma unroll
    for (int i = 0; i < 16; ++i) glds16s(wo + sib * 256 + hw * 64 + hf * 262144 + i * 16384, wvo, lds0 + L_STG + hw * 16384 + i * 1024);
  };
  auto wo_read = [&](auto HF) {
    constexpr int hf = decltype(HF)::value;
    const int G = 4 * (g4 & 1) + p4;
    const char* wa = stg + (4 * h + q4) * 64 + ((G & 1) * 4 + (G >> 1)) * 8;
    VMWAIT(0);
#pragma unroll
    for (int ks = 0; ks < 16; ++ks) wof[hf * 16 + ks] = cat(trread(wa + ks * 1024), trread(wa + ks * 1024 + 512));
    asm volatile("s_waitcnt lgkmcnt(0)" ::: "memory");
  };
  wo_stage(0);
  wo_read(IC<0>{});
  wo_stage(1);
  if (role == 0) { while (!lds_ld(ctrl + 60)) __builtin_amdgcn_s_sleep(2); scan(); }
  if (role) {
    while (!__all(lds_ld(ctrl + 356 + (lane & 3) * 4) != 0u)) __builtin_amdgcn_s_sleep(2);
    if (role == 1) {
      while (!__all(lds_ld(ctrl + 64 + (lane & 3) * 4) != 0u)) __builtin_amdgcn_s_sleep(8);
      asm volatile("" ::: "memory");
      merge(0);
    }
  }
  wo_read(IC<1>{});
  bf16x8 wdf[7];
  int fst = ffa ? 0 : 1;
  auto step_a2 = [&]() __attribute__((always_inline)) {
    asm volatile("" ::: "memory");
    const u32x4 pg = BC(u32x4, __builtin_amdgcn_raw_buffer_load_b128(rs, lane * 16, (int)WS_PG + b * 2048, 16));
    const u32x4 pu = BC(u32x4, __builtin_amdgcn_raw_buffer_load_b128(rs, lane * 16, (int)WS_PG + b * 2048 + 1024, 16));
    if (!__all(((pg[1] ^ tag) | (pg[3] ^ tag) | (pu[1] ^ tag) | (pu[3] ^ tag)) == 0u)) return;
    float v[4]; const unsigned vv[4] = {pg[0], pg[2], pu[0], pu[2]};
    const float si1 = dppf<0x55>(si), si0 = dppf<0x00>(si);
#pragma unroll
    for (int e = 0; e < 4; ++e) { float t = BC(float, vv[e]); t += dppf<0x128>(t); v[e] = rbf(halfsum(x16sum(t)) * ((e & 1) ? si1 : si0)); }
    if (lane < 8) gst64(ws + WS_HG + (size_t)(b * 8 + lane) * 8, pk2(rbf(silu_f(v[0])) * v[2], rbf(silu_f(v[1])) * v[3]), tag);
    fst = 1;
  };
  if (role == 0) {
    while (!__all(lds_ld(ctrl + (lane & 3) * 4) != 0u)) __builtin_amdgcn_s_sleep(4);
    asm volatile("" ::: "memory");
    u32x4 wdr[7];
    if (ffa) {
      const unsigned vo = (lane >> 3) * (F_ * 2) + (lane & 7) * 16;
#pragma unroll
      for (int i = 0; i < 16; ++i) glds16s((i < 8 ? wg : wu) + (size_t)(kp * 64 + (i & 7) * 8) * (F_ * 2) + cg * 128, vo, lds0 + L_STG + i * 1024);
    }
#pragma unroll
    for (int p = 0; p < 7; ++p) wdr[p] = *(const u32x4*)(wd + (size_t)fr * 229376 + cc * 32 + p * 32768 + (lane >> 1) * 1024 + (lane & 1) * 16);
    if (ffa) {
      VMWAIT(0);
      const char* ga = stg + (8 * g4 + q4) * 128 + 8 * p4;
      const char* xsa = smem + L_XS + (p4 < 2 ? p4 : 2) * 1024 + kp * 128 + g4 * 16;
      const bf16x8 xa0 = *(const bf16x8*)(xsa), xa1 = *(const bf16x8*)(xsa + 64);
      const float sm1 = dppf<0x55>(sm), sm0 = dppf<0x00>(sm);
      const f32x4 z4 = {0.f, 0.f, 0.f, 0.f};
      f32x4 d[8];
#pragma unroll
      for (int ti = 0; ti < 8; ++ti) {
        const char* g = ga + (ti >> 2) * 8192 + (ti & 3) * 32;
        d[ti] = MFMA16(xa0, cat(trread(g), trread(g + 512)), z4);
        d[ti] = MFMA16(xa1, cat(trread(g + 4096), trread(g + 4608)), d[ti]);
      }
#pragma unroll
      for (int mt = 0; mt < 2; ++mt) {
        const f32x4 e = g4 < 2 ? (g4 ? d[4 * mt + 1] : d[4 * mt]) : (g4 == 2 ? d[4 * mt + 2] : d[4 * mt + 3]);
        __builtin_amdgcn_raw_buffer_store_b128(u32x4{BC(unsigned, e[0] - sm0 * e[2]), tag, BC(unsigned, e[1] - sm1 * e[2]), tag}, rs,
                                               (lane >> 3) * 2048 + (lane & 7) * 16, (int)WS_PG + cg * 16384 + mt * 1024 + kp * 128, 16);
      }
    }
    asm volatile("s_waitcnt lgkmcnt(0)" ::: "memory");
#pragma unroll
    for (int p = 0; p < 7; ++p) *(u32x4*)(smem + L_STG + p * 1024 + lane * 16) = wdr[p];
    asm volatile("" ::: "memory");
    const char* da = stg + (8 * g4 + q4) * 32 + 8 * p4;
#pragma unroll
    for (int ks = 0; ks < 7; ++ks) wdf[ks] = cat(trread(da + ks * 1024), trread(da + ks * 1024 + 128));
    while (!__all(lds_ld(ctrl + 356 + (lane & 3) * 4) != 0u)) __builtin_amdgcn_s_sleep(2);
  }
  f32x16 sacc = zero;
  const char* c4 = ctrl + (lane & 3) * 4;
  u32x4 ga, gb;
  auto f_issue = [&](int j) {
    const int go = (int)WS_OX + ((tt * 8 + j) * 4 + fs_) * 2048;
    asm volatile("" ::: "memory");
    ga = BC(u32x4, __builtin_amdgcn_raw_buffer_load_b128(rs, lane * 16, go, 16));
    gb = BC(u32x4, __builtin_amdgcn_raw_buffer_load_b128(rs, lane * 16, go + 1024, 16));
  };
  auto f_finish = [&](int j) {
    for (unsigned spins = 0; spins < SPIN_CAP; ++spins) {
      if (__all(((ga[1] ^ tag) | (ga[3] ^ tag) | (gb[1] ^ tag) | (gb[3] ^ tag)) == 0u)) break;
      f_issue(j);
    }
    *(u32x4*)(smem + L_OB + (j * 4 + fs_) * 1024 + lane * 16) = u32x4{ga[0], ga[2], gb[0], gb[2]};
    lds_st(ctrl + 384 + (j * 4 + fs_) * 4, 1u);
  };
  auto project = [&](auto JC, auto S0, auto S1) {
    constexpr int J = decltype(JC)::value;
    while (!__all(lds_ld(c4 + 384 + J * 16) != 0u)) { if (J < 7) __builtin_amdgcn_s_sleep(2); }
    asm volatile("" ::: "memory");
    bf16x8 ob[4];
#pragma unroll
    for (int s = decltype(S0)::value; s < decltype(S1)::value; ++s) ob[s] = *(const bf16x8*)(smem + L_OB + (J * 4 + s) * 1024 + lane * 16);
#pragma unroll
    for (int s = decltype(S0)::value; s < decltype(S1)::value; ++s) {
      sacc = MFMA32(wof[8 * s + J], ob[s], sacc);
      if (J < 7) { SB; asm volatile("s_nop 13"); SB; }
    }
  };
  int own = 0;
  auto own_init = [&]() {
    while (!__builtin_amdgcn_readfirstlane(lds_ld(ctrl + 336))) __builtin_amdgcn_s_sleep(4);
    asm volatile("" ::: "memory");
    const u32x2 sl = *(const u32x2*)(ctrl + 320);
    own = ((__builtin_amdgcn_readfirstlane(sl[0]) >> 5) == tt ? 1 : 0) | ((__builtin_amdgcn_readfirstlane(sl[1]) >> 5) == tt ? 2 : 0);
  };
  int ctodo = 0;
  auto step_c = [&](unsigned tries) __attribute__((always_inline)) {
#pragma unroll 1
    for (int t0 = 0; t0 < 2; ++t0) {
      if (!((ctodo >> t0) & 1)) continue;
      float y0, y1; bool got = false;
      for (unsigned spins = 0; spins < tries; ++spins) {
        u32x4 ya[8]; unsigned bad = 0; y0 = 0.f; y1 = 0.f;
        asm volatile("" ::: "memory");
#pragma unroll
        for (int i = 0; i < 8; ++i) ya[i] = BC(u32x4, __builtin_amdgcn_raw_buffer_load_b128(rs, lane * 16, (int)WS_YG + ((i * 2 + t0) * 512 + 128 * sib) * 8, 16));
#pragma unroll
        for (int i = 0; i < 8; ++i) { const unsigned a0 = ya[i][0], a2 = ya[i][2]; bad |= (ya[i][1] ^ tag) | (ya[i][3] ^ tag); y0 += BC(float, a0); y1 += BC(float, a2); }
        if (__all(bad == 0u)) { got = true; break; }
      }
      if (!got) continue;
      const float rst = *(const float*)(ctrl + 328 + t0 * 4);
      const unsigned u = *(const unsigned*)(smem + L_XS + t0 * 1024 + sib * 256 + lane * 4);
      const float mt = t0 ? dppf<0x55>(sm) : dppf<0x00>(sm), it = t0 ? dppf<0x55>(si) : dppf<0x00>(si);
      *(unsigned*)(smem + L_YS + t0 * 256 + lane * 4) =
          pk2(silu_f(rbf(rbf((bflo(u) - mt) * it) + rbf(rbf(y0) * rst))), silu_f(rbf(rbf((bfhi(u) - mt) * it) + rbf(rbf(y1) * rst))));
      ctodo &= ~(1 << t0);
      if (!ctodo) lds_st(ctrl + 376, 1u);
    }
  };
  bool bdone = false;
  auto step_b = [&](unsigned tries) __attribute__((always_inline)) {
        unsigned long long hg[4];
        for (unsigned spins = 0; spins < tries; ++spins) {
          bool ok = true;
#pragma unroll
          for (int i = 0; i < 4; ++i) hg[i] = gld64(ws + WS_HG + (size_t)(fr * 224 + (lane < 56 ? lane : 55) * 4 + i) * 8);
#pragma unroll
          for (int i = 0; i < 4; ++i) ok &= (unsigned)(hg[i] >> 32) == tag;
          if (__all(ok)) { bdone = true; break; }
          if (tries > 1) __builtin_amdgcn_s_sleep(8);
        }
        if (!bdone && tries == 1) return;
      const unsigned h0 = (unsigned)hg[0], h1 = (unsigned)hg[1], h2 = (unsigned)hg[2], h3 = (unsigned)hg[3];
      if (lane < 56) {
        *(u32x2*)(smem + L_HS + lane * 8) = u32x2{__builtin_amdgcn_perm(h1, h0, 0x05040100u), __builtin_amdgcn_perm(h3, h2, 0x05040100u)};
        *(u32x2*)(smem + L_HS + 512 + lane * 8) = u32x2{__builtin_amdgcn_perm(h1, h0, 0x07060302u), __builtin_amdgcn_perm(h3, h2, 0x07060302u)};
      }
      asm volatile("" ::: "memory");
      f32x4 d = {0.f, 0.f, 0.f, 0.f};
      const char* ha = smem + L_HS + ((n >> 2) & 1) * 512 + g4 * 16;
#pragma unroll
      for (int ks = 0; ks < 7; ++ks) d = MFMA16(*(const bf16x8*)(ha + ks * 64), wdf[ks], d);
      if (lane < 32) gst64(ws + WS_YG + (size_t)((fr * 2 + g4) * 512 + cc * 16 + n) * 8, BC(unsigned, d[0] + 0.0f), tag);
  };
  if (role == 0) { own_init(); ctodo = own; }
  auto ffn_wait = [&](int j) __attribute__((always_inline)) {
    for (unsigned spins = 0; (!bdone || ctodo) && spins < SPIN_CAP; ++spins) {
      if (fst == 0) step_a2(); else if (!bdone) step_b(1u); else step_c(1u);
      if (j >= 0 && __all(lds_ld(c4 + (j + 2) * 32) != 0u)) break;
      __builtin_amdgcn_s_sleep(4);
    }
  };
  static_for<0, 8>([&](auto JC) {
    constexpr int j = decltype(JC)::value;
    if constexpr (j == 0 || j == 6 || j == 7) {
      if (role == 0) ffn_wait(j == 0 ? 4 : j);
    }
    if (j > 0) { while (!__all(lds_ld(c4 + (j + 2) * 32) != 0u)) __builtin_amdgcn_s_sleep(j == 7 ? 1 : 8); }
    asm volatile("" ::: "memory");
    if constexpr (j == 7) {
      if (fetch) { f_issue(7); f_finish(7); }
      project(IC<7>{}, IC<0>{}, IC<4>{});
    } else {
      if constexpr (j > 0) { if (fetch) f_issue(j - 1); }
      if (j > 0 && role == ((0x1321321 >> (4 * j)) & 3)) { merge(j); }
      if constexpr (j > 1) project(IC<(j > 1 ? j - 2 : 0)>{}, IC<0>{}, IC<4>{});
      if constexpr (j > 0) { if (fetch) f_finish(j - 1); }
    }
    if constexpr (j == 2) {
      if (fetch && lane < 32) glds4c(ws + WS_OX + (size_t)((tt * 8 + 6 + (lane >> 4)) * 4 + fs_) * 2048 + (lane & 15) * 128, lds0 + L_PF);
    }
    if constexpr (j == 5) {
      if (role) own_init();
    }
    if constexpr (j == 6) {
      if (fetch) f_issue(6);
      project(IC<5>{}, IC<0>{}, IC<4>{});
      if (fetch) f_finish(6);
      project(IC<6>{}, IC<0>{}, IC<4>{});
    }
  });
  if (own) {
    if (role == 0) { ffn_wait(-1); lds_st(ctrl + 376, 1u); }
    while (!__builtin_amdgcn_readfirstlane(lds_ld(ctrl + 376))) __builtin_amdgcn_s_sleep(1);
    asm volatile("" ::: "memory");
    const u32x2 sl = *(const u32x2*)(ctrl + 320);
    const int t0 = (int)sl[0] == tok0 + r ? 0 : 1;
    const u32x4 g0 = *(const u32x4*)(smem + L_YS + t0 * 256 + hw * 64 + h * 32), g1 = *(const u32x4*)(smem + L_YS + t0 * 256 + hw * 64 + h * 32 + 16);
    if ((int)sl[0] == tok0 + r || (int)sl[1] == tok0 + r) {
#pragma unroll
      for (int e = 0; e < 4; ++e) { gp[e] = g0[e]; gp[4 + e] = g1[e]; }
    }
  }
  if (lane < 20) lds_st(ctrl + (lane >> 1) * 32 + 16 + (lane & 1) * 4, 1u);
  {
    asm volatile("" : "+v"(x0), "+v"(x1));
    u32x4 o0, o1;
#pragma unroll
    for (int e = 0; e < 4; ++e) {
      o0[e] = pk2(sacc[2 * e] * bflo(gp[e]) + bflo(x0[e]), sacc[2 * e + 1] * bfhi(gp[e]) + bfhi(x0[e]));
      o1[e] = pk2(sacc[8 + 2 * e] * bflo(gp[4 + e]) + bflo(x1[e]), sacc[8 + 2 * e + 1] * bfhi(gp[4 + e]) + bfhi(x1[e]));
    }
    char* op = out + (size_t)(tok0 + r) * 1024 + ecol * 2;
    *(u32x4*)op = o0; *(u32x4*)(op + 16) = o1;
  }
  if (role == 0) ffn_wait(-1);
  if (b == 0 && hw == 0 && lane == 0) __hip_atomic_store((gu32*)(ws + WS_EPOCH), tag, RLX_AGENT);
}

extern "C" void kernel_launch(void* const* d_in, const int* in_sizes, int n_in, void* d_out, int out_size, void* d_ws, size_t ws_size,
                              hipStream_t stream) {
  (void)hipFuncSetAttribute((const void*)fused_mqa_mod, hipFuncAttributeMaxDynamicSharedMemorySize, L_TOTAL);
  fused_mqa_mod<<<256, 512, L_TOTAL, stream>>>((const char*)d_in[0], (const char*)d_in[1], (const char*)d_in[2], (const char*)d_in[3],
                                               (const char*)d_in[4], (const char*)d_in[6], (const char*)d_in[7], (const char*)d_in[8],
                                               (const char*)d_in[9], (char*)d_ws, (char*)d_out);
}
```
